```python
import torch

def _softmax(x, axis=-1):
    return torch.softmax(x, dim=axis)
def _gelu(x):
    return torch.nn.functional.gelu(x, approximate="tanh")
def _silu(x):
    return torch.nn.functional.silu(x)
def _sigmoid(x):
    return torch.sigmoid(x)
def _layernorm(x, axis=-1, eps=1e-5):
    m = x.mean(dim=axis, keepdim=True)
    v = x.var(dim=axis, keepdim=True, unbiased=False)
    return (x - m) / torch.sqrt(v + eps)
def _scatter_add(tgt, idx, src, axis=0):
    return tgt.clone().index_add_(axis, idx, src)
def _segment_sum(src, seg, n):
    out = torch.zeros((n,) + src.shape[1:], dtype=src.dtype)
    return out.index_add_(0, seg, src)
def _pad(x, pw):
    flat = tuple(v for pair in reversed(pw) for v in pair)
    return torch.nn.functional.pad(x, flat)

kernel_name = 'parallel_sum__spectral_gfnet__dense_activation_mish__prompt'

def setup_inputs():

    _w = (lambda a: (lambda t: t.to(torch.bfloat16) if t.is_floating_point() else t)(torch.as_tensor(a)))
    def _A_setup():
        B, S, D = 1, 2048, 1024
        Df = D // 2 + 1
        return {
            "x": _w((lambda *s: torch.randn(*s, dtype=torch.bfloat16) * 0.05)(B, S, D)),
            "w_r": _w((lambda *s: torch.randn(*s, dtype=torch.bfloat16) * 0.8)(S, Df) * 0.02),
            "w_i": _w((lambda *s: torch.randn(*s, dtype=torch.bfloat16) * 0.8)(S, Df) * 0.02),
        }
    _A_in = _A_setup()

    def _B_setup():
        B, S, D, F = 1, 2048, 1024, 3584
        return {
            "x": _w((lambda *s: torch.randn(*s, dtype=torch.bfloat16) * 0.05)(B, S, D)),
            "w1": _w((lambda *s: torch.randn(*s, dtype=torch.bfloat16) * 0.8)(D, F) * (D ** -0.5)),
            "b1": _w((lambda *s: torch.randn(*s, dtype=torch.bfloat16) * 0.2)(F)),
            "w2": _w((lambda *s: torch.randn(*s, dtype=torch.bfloat16) * 0.1)(F, D) * (F ** -0.5)),
        }
    _B_in = _B_setup()

    return {
        "A_x": _A_in["x"],
        "A_w_r": _A_in["w_r"],
        "A_w_i": _A_in["w_i"],
        "B_x": _B_in["x"],
        "B_w1": _B_in["w1"],
        "B_b1": _B_in["b1"],
        "B_w2": _B_in["w2"],
    }

def reference(A_x, A_w_r, A_w_i, B_x, B_w1, B_b1, B_w2):
    def _A_ref(x, w_r, w_i):
        B, S, D = x.shape
        X = (lambda x, axes=(-2,-1): torch.fft.rfft2(x.float(), dim=axes))(x, axes=(-2, -1))
        W = w_r + 1j * w_i
        X = X * W
        y = (lambda x, s, axes=(-2,-1): torch.fft.irfft2(x, s=s, dim=axes).to(torch.bfloat16))(X, (S, D), axes=(-2, -1))
        return y

    def _B_ref(x, w1, b1, w2):
        h = torch.einsum("bsd,df->bsf", x, w1) + b1
        sp = torch.log1p(torch.exp(-torch.abs(h))) + torch.where(h > 0, h, torch.zeros_like(h))
        act = h * torch.tanh(sp)
        return torch.einsum("bsf,fd->bsd", act, w2)

    x = A_x
    xn = _layernorm(x, axis=-1)
    return x + _A_ref(xn, A_w_r, A_w_i) + _B_ref(xn, B_w1, B_b1, B_w2)
```

```cpp
#include <hip/hip_runtime.h>
#include <stdint.h>

typedef float    f32x2  __attribute__((ext_vector_type(2)));
typedef float    f32x4  __attribute__((ext_vector_type(4)));
typedef float    f32x16 __attribute__((ext_vector_type(16)));
typedef _Float16 f16x2  __attribute__((ext_vector_type(2)));
typedef _Float16 f16x8  __attribute__((ext_vector_type(8)));
typedef __bf16   bf16x2 __attribute__((ext_vector_type(2)));
typedef __bf16   bf16x8 __attribute__((ext_vector_type(8)));
typedef short    s16x4  __attribute__((ext_vector_type(4)));
typedef short    s16x8  __attribute__((ext_vector_type(8)));
typedef unsigned u32x2  __attribute__((ext_vector_type(2)));
typedef unsigned u32x4  __attribute__((ext_vector_type(4)));
typedef __attribute__((address_space(3))) s16x4* lds_s16x4;
typedef __attribute__((address_space(3))) char*  lds_char;
typedef __amdgpu_buffer_rsrc_t rsrc_t;
#define DI __device__ __forceinline__
#define GAP() __builtin_amdgcn_sched_barrier(0)

constexpr size_t OFF_XN = 0;
constexpr size_t OFF_X1 = OFF_XN + (4u << 20);
constexpr size_t OFF_WT = OFF_X1 + (4u << 20);
constexpr size_t OFF_YB = OFF_WT + (4u << 20);
constexpr size_t OFF_X5 = OFF_YB + (4u << 20);
constexpr size_t OFF_W5 = OFF_X5 + 8192;
constexpr size_t OFF_Y5 = OFF_W5 + 8192;
constexpr size_t OFF_H  = OFF_Y5 + 8192 + 8192;
constexpr size_t H_BYTES = (size_t)64 * 224 * 1024;
constexpr size_t OFF_PT = OFF_H + H_BYTES;
constexpr size_t OFF_ST = OFF_PT + (16u << 20);

#define STAMP(ws, k, slot) do {} while (0)

DI unsigned pk_f16(float a, float b) { f32x2 v = {a, b}; return __builtin_bit_cast(unsigned, __builtin_convertvector(v, f16x2)); }
DI unsigned pk_bf16(float a, float b) { f32x2 v = {a, b}; return __builtin_bit_cast(unsigned, __builtin_convertvector(v, bf16x2)); }
DI float bf_lo(unsigned u) { return __uint_as_float(u << 16); }
DI float bf_hi(unsigned u) { return __uint_as_float(u & 0xffff0000u); }
DI float bf_round(float a) { return bf_lo(pk_bf16(a, a)); }
DI float h_lo(unsigned u) { return (float)__builtin_bit_cast(f16x2, u)[0]; }
DI float h_hi(unsigned u) { return (float)__builtin_bit_cast(f16x2, u)[1]; }
DI f32x16 mfma_h(u32x4 a, u32x4 b, f32x16 c) { return __builtin_amdgcn_mfma_f32_32x32x16_f16(__builtin_bit_cast(f16x8, a), __builtin_bit_cast(f16x8, b), c, 0, 0, 0); }
DI f32x16 mfma_b(u32x4 a, u32x4 b, f32x16 c) { return __builtin_amdgcn_mfma_f32_32x32x16_bf16(__builtin_bit_cast(bf16x8, a), __builtin_bit_cast(bf16x8, b), c, 0, 0, 0); }
DI float sin_rev(float r) { return __builtin_amdgcn_sinf(r); }
DI float cos_rev(float r) { return __builtin_amdgcn_cosf(r); }
DI int rfl(int x) { return __builtin_amdgcn_readfirstlane(x); }
DI rsrc_t mk_rsrc(const void* p, unsigned bytes) { return __builtin_amdgcn_make_buffer_rsrc((void*)p, 0, (int)bytes, 0x00020000); }
DI f32x16 zero16() { f32x16 z; for (int i = 0; i < 16; ++i) z[i] = 0.f; return z; }

template <int CTRL> DI float dpp_add(float v) {
  return v + __int_as_float(__builtin_amdgcn_update_dpp(0, __float_as_int(v), CTRL, 0xf, 0xf, true));
}
DI float red_row_h(float v) {
  v = dpp_add<0x128>(v); v = dpp_add<0x124>(v); v = dpp_add<0x122>(v); v = dpp_add<0x121>(v);
  auto r = __builtin_amdgcn_permlane32_swap(__float_as_uint(v), __float_as_uint(v), false, false);
  return __uint_as_float(r[0]) + __uint_as_float(r[1]);
}

constexpr int R_XN = 0, R_XI = 16384, R_WI = 32768, R_WSTR = 1088 * 4, R_LDS = R_WI + 4 * R_WSTR;

__global__ __launch_bounds__(512, 2) void k_rows_fwd(const unsigned* __restrict__ x, const unsigned* __restrict__ wr, const unsigned* __restrict__ wi, char* __restrict__ ws) {
  __shared__ __attribute__((aligned(16))) char sm[R_LDS];
  STAMP(ws, 0, 0);
  const int tid = threadIdx.x, lane = tid & 63, w = rfl(tid >> 6);
  const int u = blockIdx.x & 7, lo = blockIdx.x >> 3;
  const int r = lane & 31, h = lane >> 5;
  if (w < 4) {
    const int i = w, sp = 32 * (4 * u + i) + lo, sg = r >> 4, be = r & 15;
    const unsigned* xr = x + (size_t)(sp + 1024 * sg) * 512 + 16 * h + be;
    unsigned d[16];
#pragma unroll
    for (int a = 0; a < 16; ++a) d[a] = xr[32 * a];
    u32x4 Cf[2], Sf[2];
#pragma unroll
    for (int k = 0; k < 2; ++k)
#pragma unroll
      for (int j2 = 0; j2 < 4; ++j2) {
        const float t0 = (float)(((2 * (8 * k + 2 * j2) + h) * r) & 31) * (1.f / 32), t1 = (float)(((2 * (8 * k + 2 * j2 + 1) + h) * r) & 31) * (1.f / 32);
        Cf[k][j2] = pk_f16(cos_rev(t0), cos_rev(t1));
        Sf[k][j2] = pk_f16(-sin_rev(t0), -sin_rev(t1));
      }
    float Tc[2][8], Ts[2][8];
    u32x4 G[2][2];
#pragma unroll
    for (int j = 0; j < 2; ++j)
#pragma unroll
      for (int rho = 0; rho < 8; ++rho) {
        const int b = 2 * ((rho & 3) + 8 * (rho >> 2) + 4 * h) + j;
        const float t = (float)(b * r) * (1.f / 1024);
        Tc[j][rho] = cos_rev(t) * (1.f / 32); Ts[j][rho] = sin_rev(t) * (1.f / 32);
        const float tg = (float)((b * be + 8 * sg) & 31) * (1.f / 32);
        G[j][rho >> 2][rho & 3] = pk_f16(cos_rev(tg), sin_rev(tg));
      }
    const float fo = (float)sp * (1.f / 2048), fc = cos_rev(fo), fs = sin_rev(fo);
    f32x2 v[16], s2 = {0.f, 0.f};
#pragma unroll
    for (int a = 0; a < 16; ++a) { v[a] = f32x2{bf_lo(d[a]), bf_hi(d[a])}; s2 += v[a]; }
    const float mean = red_row_h(s2[0] + s2[1]) * (1.f / 1024);
    f32x2 q2 = {0.f, 0.f};
#pragma unroll
    for (int a = 0; a < 16; ++a) { const f32x2 t = v[a] - mean; q2 += t * t; }
    const float var = red_row_h(q2[0] + q2[1]) * (1.f / 1024);
    const float mb = bf_round(mean), sb = bf_round(__builtin_sqrtf(bf_round(bf_round(var) + 1e-5f))), inv = 1.f / sb;
    f32x2 n2 = {0.f, 0.f};
#pragma unroll
    for (int a = 0; a < 16; ++a) {
      const f32x2 t = (v[a] - mb) * inv;
      d[a] = pk_bf16(t[0], t[1]);
      v[a] = f32x2{bf_lo(d[a]), bf_hi(d[a])};
      n2 += v[a];
    }
#pragma unroll
    for (int al = 0; al < 4; ++al) {
      const int chunk = 2 * (16 * al + be) + h;
      *(u32x4*)(sm + R_XN + chunk * 128 + (((2 * i + sg) ^ (be & 7)) << 4)) = u32x4{d[4 * al], d[4 * al + 1], d[4 * al + 2], d[4 * al + 3]};
    }
    const float nyq = red_row_h(n2[0] - n2[1]) * (1.f / 32);
    f32x16 Ur[2], Ui[2];
#pragma unroll
    for (int j = 0; j < 2; ++j) {
      Ur[j] = zero16(); Ui[j] = zero16();
#pragma unroll
      for (int k = 0; k < 2; ++k) {
        u32x4 a;
#pragma unroll
        for (int j2 = 0; j2 < 4; ++j2) a[j2] = pk_f16(v[8 * k + 2 * j2][j], v[8 * k + 2 * j2 + 1][j]);
        Ur[j] = mfma_h(a, Cf[k], Ur[j]);
        Ui[j] = mfma_h(a, Sf[k], Ui[j]);
      }
    }
    f32x16 X[2];
#pragma unroll
    for (int s = 0; s < 2; ++s) {
      X[s] = zero16();
#pragma unroll
      for (int j = 0; j < 2; ++j)
#pragma unroll
        for (int g = 0; g < 2; ++g) {
          u32x4 b;
#pragma unroll
          for (int ii = 0; ii < 4; ++ii) {
            const int rho = 4 * g + ii;
            const float re = Ur[j][8 * s + rho], im = Ui[j][8 * s + rho], c = Tc[j][rho], sn = Ts[j][rho];
            b[ii] = pk_f16(re * c + im * sn, im * c - re * sn);
          }
          X[s] = mfma_h(G[j][g], b, X[s]);
        }
    }
    unsigned* xi = (unsigned*)(sm + R_XI) + i * 1024;
#pragma unroll
    for (int rho = 0; rho < 8; ++rho) {
      const int k = r + 32 * ((rho & 3) + 8 * (rho >> 2) + 4 * h);
      const float er = X[0][rho] + X[1][rho], ei = X[0][8 + rho] + X[1][8 + rho];
      const float dr = X[0][rho] - X[1][rho], di = X[0][8 + rho] - X[1][8 + rho];
      xi[k] = pk_f16(er, ei);
      xi[512 + k] = pk_f16(dr * fc + di * fs, di * fc - dr * fs);
    }
    const float n0 = __int_as_float(__builtin_amdgcn_readlane(__float_as_int(nyq), 0)), n1 = __int_as_float(__builtin_amdgcn_readlane(__float_as_int(nyq), 16));
    if (lane == 0) {
      unsigned* x5 = (unsigned*)(ws + OFF_X5) + (u * 32 + lo) * 4 + i;
      x5[0] = pk_f16(n0 + n1, 0.f);
      x5[1024] = pk_f16((n0 - n1) * fc, -(n0 - n1) * fs);
    }
  } else {
    const int i = w - 4, tau = lo + 32 * (4 * u + i);
    const unsigned* pr = wr + (size_t)tau * 513;
    const unsigned* pi = wi + (size_t)tau * 513;
    typedef unsigned u32x4u __attribute__((ext_vector_type(4), aligned(4)));
    u32x4 R[2], I[2];
#pragma unroll
    for (int m = 0; m < 2; ++m) { R[m] = *(const u32x4u*)(pr + 256 * m + 4 * lane); I[m] = *(const u32x4u*)(pi + 256 * m + 4 * lane); }
    const unsigned Rt = pr[512], It = pi[512];
    unsigned* wimg = (unsigned*)(sm + R_WI + i * R_WSTR);
#pragma unroll
    for (int m = 0; m < 2; ++m) {
      u32x4 a, b;
      a[0] = __builtin_amdgcn_perm(I[m][0], R[m][0], 0x05040100); a[1] = __builtin_amdgcn_perm(I[m][0], R[m][0], 0x07060302);
      a[2] = __builtin_amdgcn_perm(I[m][1], R[m][1], 0x05040100); a[3] = __builtin_amdgcn_perm(I[m][1], R[m][1], 0x07060302);
      b[0] = __builtin_amdgcn_perm(I[m][2], R[m][2], 0x05040100); b[1] = __builtin_amdgcn_perm(I[m][2], R[m][2], 0x07060302);
      b[2] = __builtin_amdgcn_perm(I[m][3], R[m][3], 0x05040100); b[3] = __builtin_amdgcn_perm(I[m][3], R[m][3], 0x07060302);
      *(u32x4*)(wimg + 512 * m + 8 * lane) = a;
      *(u32x4*)(wimg + 512 * m + 8 * lane + 4) = b;
      if (m == 1 && lane == 0) ((unsigned*)(ws + OFF_W5))[(u * 32 + lo) * 4 + i] = a[0];
    }
    if (lane == 0) {
      wimg[1024] = __builtin_amdgcn_perm(It, Rt, 0x05040100);
      ((unsigned*)(ws + OFF_W5))[1024 + (u * 32 + lo) * 4 + i] = __builtin_amdgcn_perm(It, Rt, 0x07060302);
    }
  }
  __syncthreads();
  {
    const int blkp = 128 * (u & 1) + 32 * (u >> 1) + lo;
    char* xn = ws + OFF_XN + (size_t)blkp * 16384;
#pragma unroll
    for (int m = 0; m < 2; ++m) {
      const int pos = tid + 512 * m, chunk = pos >> 3, row = pos & 7;
      *(u32x4*)(xn + pos * 16) = *(const u32x4*)(sm + R_XN + chunk * 128 + ((row ^ ((chunk >> 1) & 7)) << 4));
    }
    const unsigned* xi = (const unsigned*)(sm + R_XI);
    const unsigned* wimg = (const unsigned*)(sm + R_WI);
#pragma unroll
    for (int p = 0; p < 2; ++p) {
      const int k = tid, G = p * 128 + (k >> 2);
      const size_t off = (size_t)((G * 8 + u) * 32 + lo) * 64 + (((k & 3) ^ ((lo >> 2) & 3)) << 4);
      const int e = p * 512 + k, ew = p * 513 + k;
      *(u32x4*)(ws + OFF_X1 + off) = u32x4{xi[e], xi[1024 + e], xi[2048 + e], xi[3072 + e]};
      *(u32x4*)(ws + OFF_WT + off) = u32x4{wimg[ew], wimg[1088 + ew], wimg[2176 + ew], wimg[3264 + ew]};
    }
  }
  STAMP(ws, 0, 1);
}

struct ColConst { u32x4 MR[4], MI[4]; float Tc[16], Ts[16]; };
DI void col_consts(ColConst& c, int r, int h) {
#pragma unroll
  for (int rho = 0; rho < 16; ++rho) {
    const int e = (rho & 3) + 8 * (rho >> 2) + 4 * h;
    const float t = (float)((e * r) & 31) * (1.f / 32), cs = cos_rev(t), sn = sin_rev(t);
    c.MR[rho >> 2][rho & 3] = pk_f16(cs, sn);
    c.MI[rho >> 2][rho & 3] = pk_f16(-sn, cs);
    const float tt = (float)(e * r) * (1.f / 1024);
    c.Tc[rho] = cos_rev(tt) * (1.f / 32); c.Ts[rho] = sin_rev(tt) * (1.f / 32);
  }
}
DI void col_fft(const ColConst& c, u32x4 (&D)[4], f32x16& Vr, f32x16& Vi) {
  f32x16 Ur = zero16(), Ui = zero16();
#pragma unroll
  for (int g = 0; g < 4; ++g) { Ur = mfma_h(D[g], c.MR[g], Ur); Ui = mfma_h(D[g], c.MI[g], Ui); }
  Vr = zero16(); Vi = zero16();
#pragma unroll
  for (int g = 0; g < 4; ++g) {
    u32x4 b;
#pragma unroll
    for (int i = 0; i < 4; ++i) {
      const int rho = 4 * g + i;
      b[i] = pk_f16(Ur[rho] * c.Tc[rho] + Ui[rho] * c.Ts[rho], Ui[rho] * c.Tc[rho] - Ur[rho] * c.Ts[rho]);
    }
    Vr = mfma_h(c.MR[g], b, Vr); Vi = mfma_h(c.MI[g], b, Vi);
  }
}
DI void col_problem(const ColConst& c, u32x4 (&D)[4], const u32x4 (&W)[4], float wscale) {
  f32x16 Vr, Vi;
  col_fft(c, D, Vr, Vi);
#pragma unroll
  for (int rho = 0; rho < 16; ++rho) {
    const unsigned wv = W[rho >> 2][rho & 3];
    const float a = bf_lo(wv) * wscale, b = bf_hi(wv) * wscale;
    D[rho >> 2][rho & 3] = pk_f16(Vr[rho] * b + Vi[rho] * a, Vr[rho] * a - Vi[rho] * b);
  }
  col_fft(c, D, Vr, Vi);
#pragma unroll
  for (int rho = 0; rho < 16; ++rho) D[rho >> 2][rho & 3] = pk_f16(Vi[rho], Vr[rho]);
}

__global__ __launch_bounds__(512, 2) void k_cols(char* __restrict__ ws) {
  STAMP(ws, 1, 0);
  const int tid = threadIdx.x, lane = tid & 63, w = rfl(tid >> 6), G = blockIdx.x;
  const int r = lane & 31, h = lane >> 5;
  if (w < 4 || (G == 0 && w < 6)) {
    size_t ox, ow, oy; int gs; float wsc;
    if (w < 4) {
      const size_t o = (size_t)G * 16384 + (h * 32 + r) * 64 + ((w ^ ((r >> 2) & 3)) << 4);
      ox = OFF_X1 + o; ow = OFF_WT + o; oy = OFF_YB + o; gs = 4096;
      wsc = ((G & 127) == 0 && w == 0) ? 0.5f : 1.f;
    } else {
      const size_t o = (size_t)(w - 4) * 4096 + (h * 32 + r) * 16;
      ox = OFF_X5 + o; ow = OFF_W5 + o; oy = OFF_Y5 + o; gs = 1024; wsc = 0.5f;
    }
    u32x4 D[4], W[4];
#pragma unroll
    for (int g = 0; g < 4; ++g) { D[g] = *(const u32x4*)(ws + ox + g * gs); W[g] = *(const u32x4*)(ws + ow + g * gs); }
    ColConst c; col_consts(c, r, h);
    col_problem(c, D, W, wsc);
#pragma unroll
    for (int g = 0; g < 4; ++g) *(u32x4*)(ws + oy + g * gs) = D[g];
  }
  STAMP(ws, 1, 1);
}

DI void dma16(unsigned lds_dst, int voff, rsrc_t rs, int soff) {
  asm volatile("s_mov_b32 m0, %0\n\ts_nop 0\n\tbuffer_load_dwordx4 %1, %2, %3 offen lds" :: "s"(lds_dst), "v"(voff), "s"(rs), "s"(soff) : "memory");
}
DI void ld16(u32x4& d, int voff, rsrc_t rs, int soff) {
  asm volatile("buffer_load_dwordx4 %0, %1, %2, %3 offen" : "=v"(d) : "v"(voff), "s"(rs), "s"(soff) : "memory");
}
DI void ld8(u32x2& d, int voff, rsrc_t rs, int soff) {
  asm volatile("buffer_load_dwordx2 %0, %1, %2, %3 offen" : "=v"(d) : "v"(voff), "s"(rs), "s"(soff) : "memory");
}
template <int N> DI void wait_vm() { asm volatile("s_waitcnt vmcnt(%0)" :: "i"(N) : "memory"); }
DI void bar() { asm volatile("s_waitcnt lgkmcnt(0)\n\ts_barrier" ::: "memory"); }
DI void bar_nowait() { asm volatile("s_barrier" ::: "memory"); }
DI u32x4 tr_frag(lds_char p, int off, int tstride) {
  const s16x4 a = __builtin_amdgcn_ds_read_tr16_b64_v4i16((lds_s16x4)(p + off));
  const s16x4 b = __builtin_amdgcn_ds_read_tr16_b64_v4i16((lds_s16x4)(p + off + tstride));
  return __builtin_bit_cast(u32x4, __builtin_shufflevector(a, b, 0, 1, 2, 3, 4, 5, 6, 7));
}
DI float mish(float v) {
  const float e = __builtin_amdgcn_exp2f(__builtin_fminf(v, 30.f) * 1.44269504f);
  const float n = e * (e + 2.f);
  return v * n * __builtin_amdgcn_rcpf(n + 2.f);
}

constexpr int G1_EX = 131072, G1_LDS = 163840;
constexpr int G1_PRO = 3;
constexpr int g1_dhi(int S) {
  if (S < 0) return G1_PRO;
  if (S + G1_PRO < 8) return S + G1_PRO + 1;
  const int lim = S + 8 > 32 ? 32 : S + 8, t = 8 + 3 * (S + G1_PRO - 7);
  return t < lim ? t : lim;
}
constexpr int g1_ahi(int S) { const int t = S + G1_PRO + 1; return t < 8 ? t : 8; }
constexpr int g1_extra(int S) {
  const int c = S >> 3, st = S & 7;
  return (st == 1 ? 4 : 0) + (c >= 1 && (st == 4 || st == 7) ? 1 : 0);
}
constexpr int g1_wait(int S) {
  int pos = 0, need = 0;
  for (int j = 0; j < G1_PRO; ++j) { pos += 2; if (j == S + 2) need = pos; pos += 4; if (j == S + 1) need = pos; }
  for (int T = 0; T <= S; ++T) {
    for (int j = g1_dhi(T - 1); j < g1_dhi(T); ++j) { pos += 2; if (j == S + 2) need = pos; }
    for (int j = g1_ahi(T - 1); j < g1_ahi(T); ++j) { pos += 4; if (j == S + 1) need = pos; }
    if ((T & 7) == 1 && S == T + 6) need = pos + 4 > need ? pos + 4 : need;
    pos += g1_extra(T);
  }
  return pos - need > 63 ? 63 : pos - need;
}
struct G1 {
  u32x4 A[32];
  f32x16 acc0, acc1, pend;
  u32x4 b0n, b1n;
  u32x2 bias[4];
  unsigned pk[4];
  lds_char p0, p1, ph, pexw, pexr;
  rsrc_t rw, rb, rh, rx;
  int vdma, vbias, vh, va, rot, kap, g, col0, colh, colhk;
  unsigned ldsw;
};
DI void g1_dma(const G1& c, int j) {
  const int st = j & 7, ch = j >> 3, rs = (st + c.rot) & 7;
  const int so = (256 * (2 * c.kap + (rs >> 2)) + 2 * (4 * (rs & 3) + c.g)) * 7168 + (ch < 3 ? c.col0 * 2 + ch * 128 : c.colh * 2);
  dma16(c.ldsw + st * 8192, c.vdma, c.rw, so);
  dma16(c.ldsw + st * 8192 + 1024, c.vdma, c.rw, so + 32 * 7168);
}
DI void g1_lda(G1& c, int j) {
  const int so = (32 * c.kap + 4 * ((j + c.rot) & 7)) * 256;
#pragma unroll
  for (int kk = 0; kk < 4; ++kk) ld16(c.A[4 * j + kk], c.va, c.rx, so + kk * 256);
}
template <int S, int KK> DI void g1_step(G1& c) {
  constexpr int C = S >> 3, ST = S & 7, KS = 4 * ST + KK;
  const u32x4 b0 = c.b0n, b1 = c.b1n;
  if constexpr (C >= 1 && KS == 0) {
#pragma unroll
    for (int j = 0; j < 4; ++j) *(__attribute__((address_space(3))) f32x4*)(c.pexw + j * 1024) = f32x4{c.acc1[4 * j], c.acc1[4 * j + 1], c.acc1[4 * j + 2], c.acc1[4 * j + 3]};
    c.pend = c.acc0;
  }
  if constexpr (KS < 31) {
    c.b0n = tr_frag(C < 3 ? c.p0 : c.ph, (KS + 1) * 2048, 512);
    if constexpr (C < 3) c.b1n = tr_frag(c.p1, (KS + 1) * 2048, 512);
  } else if constexpr (C < 3) {
    c.b0n = tr_frag(C < 2 ? c.p0 : c.ph, 0, 512);
    if constexpr (C < 2) c.b1n = tr_frag(c.p1, 0, 512);
  }
  if constexpr (C == 0 && KK == 0) asm volatile("" : "+v"(c.A[KS]), "+v"(c.A[KS + 1]), "+v"(c.A[KS + 2]), "+v"(c.A[KS + 3]));
  c.acc0 = mfma_b(b0, c.A[KS], KS == 0 ? zero16() : c.acc0);
  if constexpr (KK == 0) {
#pragma unroll
    for (int j = g1_dhi(S - 1); j < g1_dhi(S); ++j) g1_dma(c, j);
  }
  if constexpr (KK == 1) {
#pragma unroll
    for (int j = g1_ahi(S - 1); j < g1_ahi(S); ++j) g1_lda(c, j);
  }
  if constexpr (C < 3) c.acc1 = mfma_b(b1, c.A[KS], KS == 0 ? zero16() : c.acc1);
  if constexpr (C >= 1) {
    if constexpr (ST == 0) {
      asm volatile("" : "+v"(c.bias[KK]));
      c.pend[4 * KK] += bf_lo(c.bias[KK][0]); c.pend[4 * KK + 1] += bf_hi(c.bias[KK][0]);
      c.pend[4 * KK + 2] += bf_lo(c.bias[KK][1]); c.pend[4 * KK + 3] += bf_hi(c.bias[KK][1]);
    }
    if constexpr (ST == 1) {
      const f32x4 t = *(__attribute__((address_space(3))) f32x4*)(c.pexr + KK * 1024);
#pragma unroll
      for (int j = 0; j < 4; ++j) c.pend[4 * KK + j] += t[j];
    }
    if constexpr (ST >= 2) {
      constexpr int half = (ST - 2) / 3, i12 = ((ST - 2) % 3) * 4 + KK;
      if constexpr (i12 % 3 == 0) c.pend[8 * half + 2 * (i12 / 3)] = mish(c.pend[8 * half + 2 * (i12 / 3)]);
      if constexpr (i12 % 3 == 1) c.pk[i12 / 3] = pk_bf16(c.pend[8 * half + 2 * (i12 / 3)], mish(c.pend[8 * half + 2 * (i12 / 3) + 1]));
      if constexpr (i12 == 11)
        __builtin_amdgcn_raw_buffer_store_b128(u32x4{c.pk[0], c.pk[1], c.pk[2], c.pk[3]}, c.rh, c.vh, (((c.col0 + 64 * (C - 1)) >> 4) + 2 * c.kap + half) * 1024, 0);
    }
  }
  if constexpr (ST == 1 && KK == 3) {
    const int so = (C < 3 ? c.col0 + 64 * C + 32 * c.kap : c.colhk + 16 * c.kap) * 2;
#pragma unroll
    for (int j = 0; j < 4; ++j) ld8(c.bias[j], c.vbias, c.rb, so + 16 * j);
  }
  if constexpr (KK == 3 && S < 30) { GAP(); wait_vm<g1_wait(S)>(); bar_nowait(); }
  GAP();
}
template <int S> DI void g1_stage(G1& c) { g1_step<S, 0>(c); g1_step<S, 1>(c); g1_step<S, 2>(c); g1_step<S, 3>(c); }
template <int S0> DI void g1_chunk(G1& c) {
  g1_stage<S0>(c); g1_stage<S0 + 1>(c); g1_stage<S0 + 2>(c); g1_stage<S0 + 3>(c);
  g1_stage<S0 + 4>(c); g1_stage<S0 + 5>(c); g1_stage<S0 + 6>(c); g1_stage<S0 + 7>(c);
}

__global__ __launch_bounds__(512, 2) void k_gemm1(const char* __restrict__ w1, const char* __restrict__ b1, char* __restrict__ ws) {
  extern __shared__ __attribute__((aligned(16))) char smem[];
  STAMP(ws, 2, 0);
  const int tid = threadIdx.x, lane = tid & 63, w = rfl(tid >> 6);
  const int xcd = blockIdx.x & 7, idx = blockIdx.x >> 3;
  const int mt = (xcd & 1) * 8 + (idx & 7), nt = (xcd >> 1) * 4 + (idx >> 3);
  const int r = lane & 31, h = lane >> 5, blk = (lane >> 4) & 1, q = (lane & 15) >> 2, p = lane & 3;
  G1 c;
  c.g = w & 3; c.kap = w >> 2; c.rot = (idx + 2 * (idx >> 3)) & 7;
  c.col0 = 224 * nt + 32 * (nt & 1);
  c.colhk = 224 * nt + ((nt & 1) ? 0 : 192);
  c.colh = c.colhk & ~63;
  c.rw = mk_rsrc(w1, 1024 * 7168);
  c.rb = mk_rsrc(b1, 3584 * 2);
  c.rx = mk_rsrc(ws + OFF_XN + (size_t)(mt * 16 + c.g * 4) * 16384, 65536);
  c.rh = mk_rsrc(ws + OFF_H + (size_t)(mt * 4 + c.g) * 224 * 1024, 224 * 1024);
  { const int jj = lane >> 3, x = lane & 7, t = jj >> 1;
    c.vdma = (64 * t + (jj & 1)) * 7168 + ((x ^ ((t & 1) << 2)) << 4); }
  c.va = (r >> 3) * 16384 + h * 128 + (r & 7) * 16;
  c.vbias = h * 8; c.vh = lane * 16;
  const lds_char L = (lds_char)smem;
  c.ldsw = (unsigned)(size_t)L + c.kap * 65536 + c.g * 2048;
  const int rowb = c.kap * 65536 + (8 * h + q) * 128 + 8 * p;
  c.p0 = L + rowb + ((c.kap ^ (q >> 1)) << 6) + 32 * blk;
  c.p1 = L + rowb + ((c.kap ^ 1 ^ (q >> 1)) << 6) + 32 * blk;
  c.ph = L + rowb + (((nt & 1) ^ (q >> 1)) << 6) + 32 * (blk ^ c.kap);
  c.pexw = L + G1_EX + w * 4096 + lane * 16;
  c.pexr = L + G1_EX + (w ^ 4) * 4096 + lane * 16;
  asm volatile("s_nop 4" ::: "memory");
#pragma unroll
  for (int j = 0; j < G1_PRO; ++j) { g1_dma(c, j); g1_lda(c, j); }
  wait_vm<g1_wait(-1)>(); bar_nowait();
  c.b0n = tr_frag(c.p0, 0, 512); c.b1n = tr_frag(c.p1, 0, 512);
  GAP();
  g1_chunk<0>(c); g1_chunk<8>(c); g1_chunk<16>(c); g1_chunk<24>(c);
#pragma unroll
  for (int j = 0; j < 2; ++j) *(__attribute__((address_space(3))) f32x4*)(c.pexw + j * 1024) = f32x4{c.acc0[8 + 4 * j], c.acc0[9 + 4 * j], c.acc0[10 + 4 * j], c.acc0[11 + 4 * j]};
  wait_vm<2>();
  asm volatile("" : "+v"(c.bias[0]), "+v"(c.bias[1]), "+v"(c.bias[2]), "+v"(c.bias[3]));
  bar();
#pragma unroll
  for (int j = 0; j < 2; ++j) {
    const f32x4 t = *(__attribute__((address_space(3))) f32x4*)(c.pexr + j * 1024);
    c.pk[2 * j]     = pk_bf16(mish(c.acc0[4 * j] + t[0] + bf_lo(c.bias[j][0])), mish(c.acc0[4 * j + 1] + t[1] + bf_hi(c.bias[j][0])));
    c.pk[2 * j + 1] = pk_bf16(mish(c.acc0[4 * j + 2] + t[2] + bf_lo(c.bias[j][1])), mish(c.acc0[4 * j + 3] + t[3] + bf_hi(c.bias[j][1])));
  }
  __builtin_amdgcn_raw_buffer_store_b128(u32x4{c.pk[0], c.pk[1], c.pk[2], c.pk[3]}, c.rh, c.vh, ((c.colhk >> 4) + c.kap) * 1024, 0);
  STAMP(ws, 2, 1);
}

constexpr int G2_NS = 5, G2_RA = 3, G2_LDS = G2_NS * 16384;
constexpr int g2_dhi(int S) { const int t = S < 0 ? G2_NS - 1 : S + G2_NS; return t < 14 ? t : 14; }
constexpr int g2_wait(int S) {
  int pos = 0, need = 0;
  for (int j = 0; j < G2_NS - 1; ++j) { pos += 2; if (j == S + 2) need = pos; if (j < G2_RA) { pos += 4; if (j == S + 1) need = pos; } }
  for (int T = 0; T <= S; ++T) {
    for (int j = g2_dhi(T - 1); j < g2_dhi(T); ++j) { pos += 2; if (j == S + 2) need = pos; }
    if (T + G2_RA < 14) { pos += 4; if (T + G2_RA == S + 1) need = pos; }
  }
  return pos - need > 63 ? 63 : pos - need;
}
struct G2 {
  u32x4 A[4 * G2_RA];
  f32x16 acc[4];
  u32x4 bn[4];
  lds_char p[4];
  rsrc_t rw, rh;
  int vdma, va;
  unsigned ldsw;
};
DI void g2_dma(const G2& c, int j) {
  dma16(c.ldsw + (j % G2_NS) * 16384, c.vdma, c.rw, j * 64 * 2048);
  dma16(c.ldsw + (j % G2_NS) * 16384 + 1024, c.vdma, c.rw, j * 64 * 2048 + 8 * 2048);
}
template <int S, int KK> DI void g2_step(G2& c) {
  constexpr int KS = 4 * S + KK, AI = 4 * (S % G2_RA) + KK;
  u32x4 b[4];
#pragma unroll
  for (int j = 0; j < 4; ++j) b[j] = c.bn[j];
  if constexpr (KK == 0) asm volatile("" : "+v"(c.A[AI]), "+v"(c.A[AI + 1]), "+v"(c.A[AI + 2]), "+v"(c.A[AI + 3]));
  constexpr int off = (((KS + 1) >> 2) % G2_NS) * 16384 + ((KS + 1) & 3) * 4096;
#pragma unroll
  for (int j = 0; j < 4; ++j) {
    if constexpr (KS < 55) c.bn[j] = tr_frag(c.p[j], off, 1024);
    c.acc[j] = mfma_b(b[j], c.A[AI], KS == 0 ? zero16() : c.acc[j]);
    if (KK == 0 && j == 0) {
#pragma unroll
      for (int d = g2_dhi(S - 1); d < g2_dhi(S); ++d) g2_dma(c, d);
    }
    GAP();
  }
  if constexpr (S + G2_RA < 14) ld16(c.A[AI], c.va, c.rh, (KS + 4 * G2_RA) * 1024);
  if constexpr (KK == 3 && S < 13) { GAP(); wait_vm<g2_wait(S)>(); if constexpr (S < 12) bar_nowait(); }
  GAP();
}
template <int S> DI void g2_stage(G2& c) { g2_step<S, 0>(c); g2_step<S, 1>(c); g2_step<S, 2>(c); g2_step<S, 3>(c); }

__global__ __launch_bounds__(512, 2) void k_gemm2(const char* __restrict__ w2, char* __restrict__ ws) {
  extern __shared__ __attribute__((aligned(16))) char smem[];
  STAMP(ws, 3, 0);
  const int tid = threadIdx.x, lane = tid & 63, w = rfl(tid >> 6);
  const int xcd = blockIdx.x & 7, idx = blockIdx.x >> 3;
  const int kq = xcd >> 1, mt = (xcd & 1) * 4 + (idx & 3), nt = idx >> 2, rg = mt * 8 + w;
  const int h = lane >> 5, blk = (lane >> 4) & 1, q = (lane & 15) >> 2, p = lane & 3;
  G2 c;
  c.rw = mk_rsrc(w2 + ((size_t)kq * 896 + (w >> 1) * 16 + (w & 1) * 4) * 2048 + nt * 256, 896 * 2048 - ((w >> 1) * 16 + (w & 1) * 4) * 2048 - nt * 256);
  c.rh = mk_rsrc(ws + OFF_H + ((size_t)rg * 224 + kq * 56) * 1024, 56 * 1024);
  { const int qq = lane >> 4, x = lane & 15; c.vdma = qq * 2048 + ((x ^ (qq << 2)) << 4); }
  c.va = lane * 16;
  const lds_char L = (lds_char)smem;
  c.ldsw = (unsigned)(size_t)L + w * 2048;
#pragma unroll
  for (int j = 0; j < 4; ++j) c.p[j] = L + (8 * h + q) * 256 + ((j ^ q) << 6) + 32 * blk + 8 * p;
  asm volatile("s_nop 4" ::: "memory");
#pragma unroll
  for (int j = 0; j < G2_NS - 1; ++j) {
    g2_dma(c, j);
    if (j < G2_RA) {
#pragma unroll
      for (int kk = 0; kk < 4; ++kk) ld16(c.A[4 * j + kk], c.va, c.rh, (4 * j + kk) * 1024);
    }
  }
  wait_vm<g2_wait(-1)>(); bar_nowait();
#pragma unroll
  for (int j = 0; j < 4; ++j) c.bn[j] = tr_frag(c.p[j], 0, 1024);
  GAP();
  g2_stage<0>(c); g2_stage<1>(c); g2_stage<2>(c); g2_stage<3>(c); g2_stage<4>(c); g2_stage<5>(c); g2_stage<6>(c);
  g2_stage<7>(c); g2_stage<8>(c); g2_stage<9>(c); g2_stage<10>(c); g2_stage<11>(c); g2_stage<12>(c); g2_stage<13>(c);
  char* pt = ws + OFF_PT + ((size_t)(kq * 64 + rg) * 32 + nt * 4) * 2048 + lane * 16;
#pragma unroll
  for (int j = 0; j < 4; ++j)
#pragma unroll
    for (int s = 0; s < 2; ++s)
      *(u32x4*)(pt + j * 2048 + s * 1024) = u32x4{pk_bf16(c.acc[j][8 * s], c.acc[j][8 * s + 1]), pk_bf16(c.acc[j][8 * s + 2], c.acc[j][8 * s + 3]),
                                                  pk_bf16(c.acc[j][8 * s + 4], c.acc[j][8 * s + 5]), pk_bf16(c.acc[j][8 * s + 6], c.acc[j][8 * s + 7])};
  STAMP(ws, 3, 1);
}

constexpr int I_Y = 0, I_P = 16384, I_LDS = I_P + 65536;
__global__ __launch_bounds__(512, 2) void k_rows_inv(const char* __restrict__ x, char* __restrict__ out, char* __restrict__ ws) {
  extern __shared__ __attribute__((aligned(16))) char smem[];
  STAMP(ws, 4, 0);
  const int tid = threadIdx.x, lane = tid & 63, w = rfl(tid >> 6);
  const int u = blockIdx.x & 7, lo = blockIdx.x >> 3, blkp = 128 * (u & 1) + 32 * (u >> 1) + lo;
  const int r = lane & 31, h = lane >> 5, i = w >> 1, sg = w & 1, sp = 32 * (4 * u + i) + lo, row = sp + 1024 * sg;
  const lds_char L = (lds_char)smem;
  const unsigned Lb = (unsigned)(size_t)L;
  {
    const rsrc_t ry = mk_rsrc(ws + OFF_YB + (u * 32 + lo) * 64, (4u << 20) - (u * 32 + lo) * 64);
    const int vy = (lane >> 2) * 16384 + (((lane & 3) ^ ((lo >> 2) & 3)) << 4);
    const rsrc_t rp = mk_rsrc(ws + OFF_PT + (size_t)(blkp >> 2) * 65536 + (blkp & 3) * 128, (16u << 20) - ((blkp >> 2) * 65536 + (blkp & 3) * 128));
    asm volatile("s_nop 4" ::: "memory");
    dma16(Lb + I_Y + w * 2048, vy, ry, w * 32 * 16384);
    dma16(Lb + I_Y + w * 2048 + 1024, vy, ry, (w * 32 + 16) * 16384);
#pragma unroll
    for (int m = 0; m < 8; ++m) {
      const int n = w * 8 + m, qq = n >> 4, a0 = (n & 15) * 2, a = a0 + (lane >> 5), ln = ((lane >> 3) & 3) ^ ((a >> 3) & 1), rw = (lane & 7) ^ (a & 7);
      dma16(Lb + I_P + n * 1024, (lane >> 5) * 2048 + ln * 512 + rw * 16, rp, qq * (4 << 20) + a0 * 2048);
    }
  }
  u32x2 xr[4];
  const rsrc_t rx = mk_rsrc(x + (size_t)row * 2048, 2048);
  u32x2 y5;
  const rsrc_t r5 = mk_rsrc(ws + OFF_Y5, 8192);
  asm volatile("s_nop 4" ::: "memory");
#pragma unroll
  for (int g = 0; g < 4; ++g) ld8(xr[g], r * 64 + h * 8, rx, g * 16);
  asm volatile("buffer_load_dword %0, %1, %2, 0 offen" : "=v"(y5[0]) : "v"(((u * 32 + lo) * 4 + i) * 4), "s"(r5) : "memory");
  asm volatile("buffer_load_dword %0, %1, %2, 0 offen" : "=v"(y5[1]) : "v"(((u * 32 + lo) * 4 + i) * 4 + 4096), "s"(r5) : "memory");
  u32x4 MR[4], MI[2]; float Tc[16], Ts[16];
#pragma unroll
  for (int rho = 0; rho < 16; ++rho) {
    const int e = (rho & 3) + 8 * (rho >> 2) + 4 * h;
    const float t = (float)((e * r) & 31) * (1.f / 32), cs = cos_rev(t), sn = sin_rev(t);
    MR[rho >> 2][rho & 3] = pk_f16(cs, -sn);
    if (rho < 8) MI[rho >> 2][rho & 3] = pk_f16(sn, cs);
    const float tt = (float)(e * r) * (1.f / 1024);
    Tc[rho] = cos_rev(tt) * (1.f / 32); Ts[rho] = sin_rev(tt) * (1.f / 32);
  }
  const float fo = (float)sp * (1.f / 2048), sgn = sg ? -1.f : 1.f, fc = cos_rev(fo) * sgn, fs = sin_rev(fo) * sgn;
  wait_vm<14>(); bar_nowait();
  u32x4 D[2];
#pragma unroll
  for (int rho = 0; rho < 8; ++rho) {
    const int k = r + 32 * ((rho & 3) + 8 * (rho >> 2) + 4 * h);
    const unsigned ya = *(__attribute__((address_space(3))) unsigned*)(L + I_Y + k * 16 + i * 4);
    const unsigned yb = *(__attribute__((address_space(3))) unsigned*)(L + I_Y + 8192 + k * 16 + i * 4);
    const float br = h_lo(yb), bi = h_hi(yb);
    D[rho >> 2][rho & 3] = pk_f16(h_lo(ya) + br * fc - bi * fs, h_hi(ya) + bi * fc + br * fs);
  }
  f32x16 Vr = zero16(), Vi = zero16();
#pragma unroll
  for (int k = 0; k < 2; ++k) { Vr = mfma_h(D[k], MR[k], Vr); Vi = mfma_h(D[k], MI[k], Vi); }
  f32x16 y = zero16();
#pragma unroll
  for (int g = 0; g < 4; ++g) {
    u32x4 a;
#pragma unroll
    for (int ii = 0; ii < 4; ++ii) {
      const int rho = 4 * g + ii;
      a[ii] = pk_f16(Vr[rho] * Tc[rho] - Vi[rho] * Ts[rho], Vi[rho] * Tc[rho] + Vr[rho] * Ts[rho]);
    }
    y = mfma_h(a, MR[g], y);
  }
  wait_vm<0>();
  asm volatile("" : "+v"(xr[0]), "+v"(xr[1]), "+v"(xr[2]), "+v"(xr[3]), "+v"(y5));
  bar_nowait();
  const float nyq = (h_lo(y5[0]) + h_lo(y5[1]) * fc - h_hi(y5[1]) * fs) * (1.f / 32);
  float o[16];
#pragma unroll
  for (int g = 0; g < 4; ++g) {
    o[4 * g]     = bf_round(bf_lo(xr[g][0]) + bf_round(y[4 * g] + nyq));
    o[4 * g + 1] = bf_round(bf_hi(xr[g][0]) + bf_round(y[4 * g + 1] - nyq));
    o[4 * g + 2] = bf_round(bf_lo(xr[g][1]) + bf_round(y[4 * g + 2] + nyq));
    o[4 * g + 3] = bf_round(bf_hi(xr[g][1]) + bf_round(y[4 * g + 3] - nyq));
  }
  float m[16];
#pragma unroll
  for (int j = 0; j < 16; ++j) m[j] = 0.f;
#pragma unroll
  for (int qq = 0; qq < 4; ++qq)
#pragma unroll
    for (int s = 0; s < 2; ++s) {
      const u32x4 t = *(__attribute__((address_space(3))) u32x4*)(L + I_P + (qq * 32 + r) * 512 + (((2 * s + h) ^ ((r >> 3) & 1)) << 7) + (((2 * i + sg) ^ (r & 7)) << 4));
#pragma unroll
      for (int j = 0; j < 4; ++j) { m[8 * s + 2 * j] += bf_lo(t[j]); m[8 * s + 2 * j + 1] += bf_hi(t[j]); }
    }
  char* orow = out + (size_t)row * 2048 + r * 64 + h * 8;
#pragma unroll
  for (int g = 0; g < 4; ++g)
    *(u32x2*)(orow + g * 16) = u32x2{pk_bf16(o[4 * g] + m[4 * g], o[4 * g + 1] + m[4 * g + 1]), pk_bf16(o[4 * g + 2] + m[4 * g + 2], o[4 * g + 3] + m[4 * g + 3])};
  STAMP(ws, 4, 1);
}

extern "C" void kernel_launch(void* const* d_in, const int* in_sizes, int n_in, void* d_out, int out_size, void* d_ws, size_t ws_size, hipStream_t stream) {
  (void)in_sizes; (void)n_in; (void)out_size; (void)ws_size;
  char* ws = (char*)d_ws;
  (void)hipFuncSetAttribute((const void*)k_gemm1, hipFuncAttributeMaxDynamicSharedMemorySize, G1_LDS);
  (void)hipFuncSetAttribute((const void*)k_gemm2, hipFuncAttributeMaxDynamicSharedMemorySize, G2_LDS);
  (void)hipFuncSetAttribute((const void*)k_rows_inv, hipFuncAttributeMaxDynamicSharedMemorySize, I_LDS);
  k_rows_fwd<<<256, 512, 0, stream>>>((const unsigned*)d_in[0], (const unsigned*)d_in[1], (const unsigned*)d_in[2], ws);
  k_cols<<<256, 512, 0, stream>>>(ws);
  k_gemm1<<<256, 512, G1_LDS, stream>>>((const char*)d_in[4], (const char*)d_in[5], ws);
  k_gemm2<<<256, 512, G2_LDS, stream>>>((const char*)d_in[6], ws);
  k_rows_inv<<<256, 512, I_LDS, stream>>>((const char*)d_in[0], (char*)d_out, ws);
}
```

```cpp
#include <hip/hip_runtime.h>
#include <stdint.h>

typedef float    f32x2  __attribute__((ext_vector_type(2)));
typedef float    f32x4  __attribute__((ext_vector_type(4)));
typedef float    f32x16 __attribute__((ext_vector_type(16)));
typedef _Float16 f16x2  __attribute__((ext_vector_type(2)));
typedef _Float16 f16x8  __attribute__((ext_vector_type(8)));
typedef __bf16   bf16x2 __attribute__((ext_vector_type(2)));
typedef __bf16   bf16x8 __attribute__((ext_vector_type(8)));
typedef short    s16x4  __attribute__((ext_vector_type(4)));
typedef short    s16x8  __attribute__((ext_vector_type(8)));
typedef unsigned u32x2  __attribute__((ext_vector_type(2)));
typedef unsigned u32x4  __attribute__((ext_vector_type(4)));
typedef __attribute__((address_space(3))) s16x4* lds_s16x4;
typedef __attribute__((address_space(3))) char*  lds_char;
typedef __amdgpu_buffer_rsrc_t rsrc_t;
#define DI __device__ __forceinline__
#define GAP() __builtin_amdgcn_sched_barrier(0)

#include <utility>
#include <type_traits>
constexpr unsigned OFF_XN = 0;
constexpr unsigned OFF_X1 = OFF_XN + (4u << 20);
constexpr unsigned OFF_WT = OFF_X1 + (4u << 20);
constexpr unsigned OFF_YB = OFF_WT + (4u << 20);
constexpr unsigned OFF_YX = OFF_YB + (4u << 20);
constexpr unsigned OFF_X5 = OFF_YX + (4u << 20);
constexpr unsigned OFF_W5 = OFF_X5 + 8192;
constexpr unsigned OFF_Y5 = OFF_W5 + 8192;
constexpr unsigned OFF_EP = OFF_Y5 + 8192;
constexpr unsigned OFF_FA = OFF_EP + 8192;
constexpr unsigned OFF_FY = OFF_FA + 8192;
constexpr unsigned OFF_FH = OFF_FY + 8192;
constexpr unsigned OFF_FP = OFF_FH + 8192;
constexpr unsigned OFF_H  = OFF_FP + 8192;
constexpr unsigned H_BYTES = 64u * 224 * 1024;
constexpr unsigned OFF_PT = OFF_H + H_BYTES;
constexpr unsigned OFF_ST = OFF_PT + (16u << 20);
constexpr unsigned WS_USED = OFF_ST;
constexpr int SC1 = 16;
#define STAMP_DECL do {} while (0)
#define STAMP(slot) do {} while (0)
#define STAMP_FLUSH(ws, k) do {} while (0)
#define STAMPC(slot) do {} while (0)

DI unsigned pk_f16(float a, float b) { f32x2 v = {a, b}; return __builtin_bit_cast(unsigned, __builtin_convertvector(v, f16x2)); }
DI unsigned pk_bf16(float a, float b) { f32x2 v = {a, b}; return __builtin_bit_cast(unsigned, __builtin_convertvector(v, bf16x2)); }
DI float bf_lo(unsigned u) { return __uint_as_float(u << 16); }
DI float bf_hi(unsigned u) { return __uint_as_float(u & 0xffff0000u); }
DI float bf_round(float a) { return bf_lo(pk_bf16(a, a)); }
DI float h_lo(unsigned u) { return (float)__builtin_bit_cast(f16x2, u)[0]; }
DI float h_hi(unsigned u) { return (float)__builtin_bit_cast(f16x2, u)[1]; }
DI f32x16 mfma_h(u32x4 a, u32x4 b, f32x16 c) { return __builtin_amdgcn_mfma_f32_32x32x16_f16(__builtin_bit_cast(f16x8, a), __builtin_bit_cast(f16x8, b), c, 0, 0, 0); }
DI f32x16 mfma_b(u32x4 a, u32x4 b, f32x16 c) {
  return __builtin_amdgcn_mfma_f32_32x32x16_bf16(__builtin_bit_cast(bf16x8, a), __builtin_bit_cast(bf16x8, b), c, 0, 0, 0); }
DI float sin_rev(float r) { return __builtin_amdgcn_sinf(r); }
DI float cos_rev(float r) { return __builtin_amdgcn_cosf(r); }
DI int rfl(int x) { return __builtin_amdgcn_readfirstlane(x); }
DI rsrc_t mk_rsrc(const void* p, unsigned bytes) { return __builtin_amdgcn_make_buffer_rsrc((void*)p, 0, (int)bytes, 0x00020000); }
DI f32x16 zero16() { f32x16 z; for (int i = 0; i < 16; ++i) z[i] = 0.f; return z; }

template <int CTRL> DI float dpp_add(float v) {
  return v + __int_as_float(__builtin_amdgcn_update_dpp(0, __float_as_int(v), CTRL, 0xf, 0xf, true));
}
DI float red_row_h(float v) {
  v = dpp_add<0x128>(v); v = dpp_add<0x124>(v); v = dpp_add<0x122>(v); v = dpp_add<0x121>(v);
  auto r = __builtin_amdgcn_permlane32_swap(__float_as_uint(v), __float_as_uint(v), false, false);
  return __uint_as_float(r[0]) + __uint_as_float(r[1]);
}

template <class F, int... I> DI void static_for_impl(F&& f, std::integer_sequence<int, I...>) { (f(std::integral_constant<int, I>{}), ...); }
template <int N, class F> DI void static_for(F&& f) { static_for_impl(f, std::make_integer_sequence<int, N>{}); }
#define PIN(x) asm volatile("" : "+v"(x))
typedef __attribute__((address_space(3))) u32x4* lds_u32x4;
typedef __attribute__((address_space(3))) u32x2* lds_u32x2;
typedef __attribute__((address_space(3))) unsigned* lds_u32;
typedef __attribute__((address_space(1))) unsigned long long gu64;

DI void setm0(unsigned v) { asm volatile("s_mov_b32 m0, %0\n\ts_nop 0" :: "s"(v) : "memory"); }
template <int OFF> DI void dma_s(int voff, rsrc_t rs, int soff) {
  asm volatile("buffer_load_dwordx4 %0, %1, %2 offen offset:%3 sc1 lds" :: "v"(voff), "s"(rs), "s"(soff), "i"(OFF) : "memory");
}
DI void dma_w(int voff, rsrc_t rs, int soff) {
  asm volatile("buffer_load_dwordx4 %0, %1, %2 offen lds" :: "v"(voff), "s"(rs), "s"(soff) : "memory");
}
template <int N> DI void wait_vm() { asm volatile("s_waitcnt vmcnt(%0)" :: "i"(N) : "memory"); }
DI void bar() { asm volatile("s_waitcnt lgkmcnt(0)\n\ts_barrier" ::: "memory"); }
DI void bar_nowait() { asm volatile("s_barrier" ::: "memory"); }
DI void st16(u32x4 v, rsrc_t rs, int voff, int soff) { __builtin_amdgcn_raw_buffer_store_b128(v, rs, voff, soff, SC1); }
DI u32x4 ld16s(rsrc_t rs, int voff, int soff) { return __builtin_amdgcn_raw_buffer_load_b128(rs, voff, soff, SC1); }
DI void mf16w(f32x4& acc, u32x4 a, u32x4 b, u32x4 other) { asm volatile("v_mfma_f32_16x16x32_bf16 %0, %1, %2, %0" : "+v"(acc) : "a"(a), "v"(b), "v"(other)); }
DI void mf16(f32x4& acc, u32x4 a, u32x4 b) { asm volatile("v_mfma_f32_16x16x32_bf16 %0, %1, %2, %0" : "+v"(acc) : "a"(a), "v"(b)); }
DI void mf16z(f32x4& acc, u32x4 a, u32x4 b) { asm volatile("v_mfma_f32_16x16x32_bf16 %0, %1, %2, 0" : "=&v"(acc) : "a"(a), "v"(b)); }
DI void mf16c(f32x4& acc, u32x4 a, u32x4 b, f32x4 c0) { asm volatile("v_mfma_f32_16x16x32_bf16 %0, %1, %2, %3" : "=&v"(acc) : "a"(a), "v"(b), "v"(c0)); }

struct Ctx {
  char* ws; rsrc_t rws; lds_char L; unsigned Lb, tlo, thi;
  int lane, w, u, lo, blkp, mt, kq;
};
DI void publish(const Ctx& c, unsigned off, unsigned count) {
  if (c.lane == 0) __hip_atomic_store((gu64*)(c.ws + off), ((unsigned long long)(c.thi + count) << 32) | c.tlo, __ATOMIC_RELAXED, __HIP_MEMORY_SCOPE_AGENT);
}
DI void wait_flags(const Ctx& c, unsigned off, bool active, unsigned need) {
  for (unsigned spins = 0; spins < (1u << 20); ++spins) {
    const unsigned long long v = __hip_atomic_load((gu64*)(c.ws + off), __ATOMIC_RELAXED, __HIP_MEMORY_SCOPE_AGENT);
    const unsigned cnt = (unsigned)(v >> 32) - c.thi;
    if (__all(!active || ((unsigned)v == c.tlo && cnt >= need && cnt < 16u))) break;
    __builtin_amdgcn_s_sleep(2);
  }
  asm volatile("" ::: "memory");
}
DI void copy_out_rows(const Ctx& c, int img, unsigned dst) {
  const int so = dst + (c.blkp >> 2) * 65536 + ((c.blkp >> 1) & 1) * 1024 + (c.blkp & 1) * 128;
#pragma unroll
  for (int m = 0; m < 4; ++m) {
    const int pc = 32 * c.w + 8 * m + (c.lane >> 3), row = c.lane & 7;
    const u32x4 v = *(lds_u32x4)(c.L + img + pc * 128 + ((row ^ ((pc >> 1) & 7)) << 4));
    st16(v, c.rws, (pc >> 2) * 2048 + (pc & 3) * 256 + row * 16, so);
  }
}

constexpr int R_STR = 1088 * 4, R_XN = 0, R_XI = 16384, R_WI = R_XI + 4 * R_STR;
DI void phase_front(Ctx& c, const unsigned* __restrict__ x, const unsigned* __restrict__ wr, const unsigned* __restrict__ wi) {
  const int lane = c.lane, i = c.w, u = c.u, lo = c.lo, r = lane & 31, h = lane >> 5;
  const int sp = 32 * (4 * u + i) + lo, sg = r >> 4, be = r & 15;
  const unsigned* xr = x + (size_t)(sp + 1024 * sg) * 512 + 16 * h + be;
  unsigned d[16];
#pragma unroll
  for (int a = 0; a < 16; ++a) d[a] = xr[32 * a];
  const unsigned long long ep = __hip_atomic_load((gu64*)(c.ws + OFF_EP), __ATOMIC_RELAXED, __HIP_MEMORY_SCOPE_AGENT);
  typedef unsigned u32x4u __attribute__((ext_vector_type(4), aligned(4)));
  const unsigned* pr = wr + (size_t)sp * 513;
  const unsigned* pi = wi + (size_t)sp * 513;
  u32x4 R[2], I[2];
#pragma unroll
  for (int m = 0; m < 2; ++m) { R[m] = *(const u32x4u*)(pr + 256 * m + 4 * lane); I[m] = *(const u32x4u*)(pi + 256 * m + 4 * lane); }
  const unsigned Rt = pr[512], It = pi[512];
  u32x4 Cf[2], Sf[2];
#pragma unroll
  for (int k = 0; k < 2; ++k)
#pragma unroll
    for (int j2 = 0; j2 < 4; ++j2) {
      const float t0 = (float)(((2 * (8 * k + 2 * j2) + h) * r) & 31) * (1.f / 32), t1 = (float)(((2 * (8 * k + 2 * j2 + 1) + h) * r) & 31) * (1.f / 32);
      Cf[k][j2] = pk_f16(cos_rev(t0), cos_rev(t1));
      Sf[k][j2] = pk_f16(-sin_rev(t0), -sin_rev(t1));
    }
  float Tc[2][8], Ts[2][8];
  u32x4 G[2][2];
#pragma unroll
  for (int j = 0; j < 2; ++j)
#pragma unroll
    for (int rho = 0; rho < 8; ++rho) {
      const int b = 2 * ((rho & 3) + 8 * (rho >> 2) + 4 * h) + j;
      const float t = (float)(b * r) * (1.f / 1024);
      Tc[j][rho] = cos_rev(t) * (1.f / 32); Ts[j][rho] = sin_rev(t) * (1.f / 32);
      const float tg = (float)((b * be + 8 * sg) & 31) * (1.f / 32);
      G[j][rho >> 2][rho & 3] = pk_f16(cos_rev(tg), sin_rev(tg));
    }
  const float fo = (float)sp * (1.f / 2048), fc = cos_rev(fo), fs = sin_rev(fo);
  f32x2 v[16], s2 = {0.f, 0.f};
#pragma unroll
  for (int a = 0; a < 16; ++a) { v[a] = f32x2{bf_lo(d[a]), bf_hi(d[a])}; s2 += v[a]; }
  const float mean = red_row_h(s2[0] + s2[1]) * (1.f / 1024);
  f32x2 q2 = {0.f, 0.f};
#pragma unroll
  for (int a = 0; a < 16; ++a) { const f32x2 t = v[a] - mean; q2 += t * t; }
  const float var = red_row_h(q2[0] + q2[1]) * (1.f / 1024);
  const float mb = bf_round(mean), sb = bf_round(__builtin_sqrtf(bf_round(bf_round(var) + 1e-5f))), inv = 1.f / sb;
  f32x2 n2 = {0.f, 0.f};
#pragma unroll
  for (int a = 0; a < 16; ++a) {
    v[a] = (v[a] - mb) * inv;
    const f32x2 t = v[a] * 1.44269504f;
    d[a] = pk_bf16(t[0], t[1]);
    n2 += v[a];
  }
#pragma unroll
  for (int al = 0; al < 4; ++al) {
    const int pc = 2 * (16 * al + be) + h;
    *(lds_u32x4)(c.L + R_XN + pc * 128 + (((2 * i + sg) ^ (be & 7)) << 4)) = u32x4{d[4 * al], d[4 * al + 1], d[4 * al + 2], d[4 * al + 3]};
  }
  c.tlo = rfl((unsigned)ep + 1u); c.thi = rfl((unsigned)(ep >> 32));
  bar();
  copy_out_rows(c, R_XN, OFF_XN);
  const float nyq = red_row_h(n2[0] - n2[1]) * (1.f / 32);
  {
    const lds_char wimg = c.L + R_WI + i * R_STR;
#pragma unroll
    for (int m = 0; m < 2; ++m) {
      u32x4 a, b;
      a[0] = __builtin_amdgcn_perm(I[m][0], R[m][0], 0x05040100); a[1] = __builtin_amdgcn_perm(I[m][0], R[m][0], 0x07060302);
      a[2] = __builtin_amdgcn_perm(I[m][1], R[m][1], 0x05040100); a[3] = __builtin_amdgcn_perm(I[m][1], R[m][1], 0x07060302);
      b[0] = __builtin_amdgcn_perm(I[m][2], R[m][2], 0x05040100); b[1] = __builtin_amdgcn_perm(I[m][2], R[m][2], 0x07060302);
      b[2] = __builtin_amdgcn_perm(I[m][3], R[m][3], 0x05040100); b[3] = __builtin_amdgcn_perm(I[m][3], R[m][3], 0x07060302);
      *(lds_u32x4)(wimg + 2048 * m + 32 * lane) = a;
      *(lds_u32x4)(wimg + 2048 * m + 32 * lane + 16) = b;
    }
    if (lane == 0) *(lds_u32x2)(wimg + 4096) = u32x2{__builtin_amdgcn_perm(It, Rt, 0x05040100), __builtin_amdgcn_perm(It, Rt, 0x07060302)};
  }
  f32x16 Ur[2], Ui[2];
#pragma unroll
  for (int j = 0; j < 2; ++j) {
    Ur[j] = zero16(); Ui[j] = zero16();
#pragma unroll
    for (int k = 0; k < 2; ++k) {
      u32x4 a;
#pragma unroll
      for (int j2 = 0; j2 < 4; ++j2) a[j2] = pk_f16(v[8 * k + 2 * j2][j], v[8 * k + 2 * j2 + 1][j]);
      Ur[j] = mfma_h(a, Cf[k], Ur[j]);
      Ui[j] = mfma_h(a, Sf[k], Ui[j]);
    }
  }
  wait_vm<0>();
  publish(c, OFF_FA + (i * 256 + c.blkp) * 8, 1);
  f32x16 X[2];
#pragma unroll
  for (int s = 0; s < 2; ++s) {
    X[s] = zero16();
#pragma unroll
    for (int j = 0; j < 2; ++j)
#pragma unroll
      for (int g = 0; g < 2; ++g) {
        u32x4 b;
#pragma unroll
        for (int ii = 0; ii < 4; ++ii) {
          const int rho = 4 * g + ii;
          const float re = Ur[j][8 * s + rho], im = Ui[j][8 * s + rho], cs = Tc[j][rho], sn = Ts[j][rho];
          b[ii] = pk_f16(re * cs + im * sn, im * cs - re * sn);
        }
        X[s] = mfma_h(G[j][g], b, X[s]);
      }
  }
  const lds_u32 xi = (lds_u32)(c.L + R_XI + i * R_STR);
#pragma unroll
  for (int rho = 0; rho < 8; ++rho) {
    const int k = r + 32 * ((rho & 3) + 8 * (rho >> 2) + 4 * h);
    const float er = X[0][rho] + X[1][rho], ei = X[0][8 + rho] + X[1][8 + rho];
    const float dr = X[0][rho] - X[1][rho], di = X[0][8 + rho] - X[1][8 + rho];
    xi[k] = pk_f16(er, ei);
    xi[513 + k] = pk_f16(dr * fc + di * fs, di * fc - dr * fs);
  }
  const float n0 = __int_as_float(__builtin_amdgcn_readlane(__float_as_int(nyq), 0)), n1 = __int_as_float(__builtin_amdgcn_readlane(__float_as_int(nyq), 16));
  if (lane == 0) { xi[512] = pk_f16(n0 + n1, 0.f); xi[1025] = pk_f16((n0 - n1) * fc, -(n0 - n1) * fs); }
  bar();
  {
    const lds_u32 xa = (lds_u32)(c.L + R_XI), wa = (lds_u32)(c.L + R_WI);
    const int p = i >> 1;
#pragma unroll
    for (int m = 0; m < 4; ++m) {
      const int prob = 256 * i + 64 * m + lane, e = prob + p;
      const int vo = (prob >> 3) * 32768 + (((prob & 7) ^ ((lo >> 1) & 7)) << 4), so = (u * 32 + lo) * 128;
      st16(u32x4{xa[e], xa[1088 + e], xa[2176 + e], xa[3264 + e]}, c.rws, vo, OFF_X1 + so);
      st16(u32x4{wa[e], wa[1088 + e], wa[2176 + e], wa[3264 + e]}, c.rws, vo, OFF_WT + so);
    }
    if ((i & 1) == 0 && lane == 0) {
      const int e = 512 + 513 * p, so = p * 4096 + (u * 32 + lo) * 16;
      st16(u32x4{xa[e], xa[1088 + e], xa[2176 + e], xa[3264 + e]}, c.rws, 0, OFF_X5 + so);
      st16(u32x4{wa[e], wa[1088 + e], wa[2176 + e], wa[3264 + e]}, c.rws, 0, OFF_W5 + so);
    }
  }
  wait_vm<0>();
  publish(c, OFF_FA + (i * 256 + c.blkp) * 8, 2);
  bar();
}

struct ColConst { u32x4 MR[4], MI[4]; float Tc[16], Ts[16]; };
DI void col_consts(ColConst& c, int r, int h) {
#pragma unroll
  for (int rho = 0; rho < 16; ++rho) {
    const int e = (rho & 3) + 8 * (rho >> 2) + 4 * h;
    const float t = (float)((e * r) & 31) * (1.f / 32), cs = cos_rev(t), sn = sin_rev(t);
    c.MR[rho >> 2][rho & 3] = pk_f16(cs, sn);
    c.MI[rho >> 2][rho & 3] = pk_f16(-sn, cs);
    const float tt = (float)(e * r) * (1.f / 1024);
    c.Tc[rho] = cos_rev(tt) * (1.f / 32); c.Ts[rho] = sin_rev(tt) * (1.f / 32);
  }
}
DI void col_fft(const ColConst& c, u32x4 (&D)[4], f32x16& Vr, f32x16& Vi) {
  f32x16 Ur = zero16(), Ui = zero16();
#pragma unroll
  for (int g = 0; g < 4; ++g) { Ur = mfma_h(D[g], c.MR[g], Ur); Ui = mfma_h(D[g], c.MI[g], Ui); }
  Vr = zero16(); Vi = zero16();
#pragma unroll
  for (int g = 0; g < 4; ++g) {
    u32x4 b;
#pragma unroll
    for (int i = 0; i < 4; ++i) {
      const int rho = 4 * g + i;
      b[i] = pk_f16(Ur[rho] * c.Tc[rho] + Ui[rho] * c.Ts[rho], Ui[rho] * c.Tc[rho] - Ur[rho] * c.Ts[rho]);
    }
    Vr = mfma_h(c.MR[g], b, Vr); Vi = mfma_h(c.MI[g], b, Vi);
  }
}
DI void col_problem(const ColConst& c, u32x4 (&D)[4], const u32x4 (&W)[4], float wscale) {
  f32x16 Vr, Vi;
  col_fft(c, D, Vr, Vi);
#pragma unroll
  for (int rho = 0; rho < 16; ++rho) {
    const unsigned wv = W[rho >> 2][rho & 3];
    const float a = bf_lo(wv) * wscale, b = bf_hi(wv) * wscale;
    D[rho >> 2][rho & 3] = pk_f16(Vr[rho] * b + Vi[rho] * a, Vr[rho] * a - Vi[rho] * b);
  }
  col_fft(c, D, Vr, Vi);
#pragma unroll
  for (int rho = 0; rho < 16; ++rho) D[rho >> 2][rho & 3] = pk_f16(Vi[rho], Vr[rho]);
}

DI void phase_cols(const Ctx& c, int j) {
  const int lane = c.lane, w = c.w, r = lane & 31, h = lane >> 5;
  ColConst k; col_consts(k, r, h);
  { const int uu = 2 * w + h; wait_flags(c, OFF_FA + ((j >> 3) * 256 + 128 * (uu & 1) + 32 * (uu >> 1) + r) * 8, true, 2); }
  auto fetch = [&](int rd) {
    const int so = (4 * j + rd) * 32768 + w * 8192;
    const unsigned lb = c.Lb + (rd & 1) * 65536 + w * 8192;
#pragma unroll
    for (int q = 0; q < 4; ++q) {
      setm0(lb + (q >> 1) * 32768 + (q & 1) * 4096);
      const int s2 = so + ((q >> 1) ? OFF_WT : OFF_X1) + (q & 1) * 4096;
      dma_s<0>(lane * 16, c.rws, s2); dma_s<1024>(lane * 16, c.rws, s2); dma_s<2048>(lane * 16, c.rws, s2); dma_s<3072>(lane * 16, c.rws, s2);
    }
  };
  fetch(0);
  bar();
  if (j == 0 && w == 0 && lane == 0)
    __hip_atomic_store((gu64*)(c.ws + OFF_EP), ((unsigned long long)(c.thi + 0x9e3779b1u) << 32) | c.tlo, __ATOMIC_RELAXED, __HIP_MEMORY_SCOPE_AGENT);
  if ((j & 15) == 0 && w == 0) {
    const int so = (j >> 4) * 4096;
    u32x4 D[4], W[4];
#pragma unroll
    for (int g = 0; g < 4; ++g) { D[g] = ld16s(c.rws, lane * 16 + g * 1024, OFF_X5 + so); W[g] = ld16s(c.rws, lane * 16 + g * 1024, OFF_W5 + so); }
    col_problem(k, D, W, 0.5f);
#pragma unroll
    for (int g = 0; g < 4; ++g) st16(D[g], c.rws, lane * 16 + g * 1024, OFF_Y5 + so);
  }
  for (int rd = 0; rd < 4; ++rd) {
    wait_vm<0>(); bar();
    if (rd < 3) fetch(rd + 1);
    const lds_char B = c.L + (rd & 1) * 65536;
#pragma unroll
    for (int e = 0; e < 2; ++e) {
      const lds_char p = B + lane * 128 + (((2 * w + e) ^ ((r >> 1) & 7)) << 4);
      u32x4 D[4], W[4];
#pragma unroll
      for (int g = 0; g < 4; ++g) { D[g] = *(lds_u32x4)(p + g * 8192); W[g] = *(lds_u32x4)(p + 32768 + g * 8192); }
      col_problem(k, D, W, ((j & 15) == 0 && rd == 0 && w == 0 && e == 0) ? 0.5f : 1.f);
#pragma unroll
      for (int g = 0; g < 4; ++g) *(lds_u32x4)(p + g * 8192) = D[g];
    }
    bar();
#pragma unroll
    for (int m = 0; m < 8; ++m)
      st16(*(lds_u32x4)(B + w * 8192 + m * 1024 + lane * 16), c.rws, lane * 16 + m * 1024, OFF_YB + (4 * j + rd) * 32768 + w * 8192);
  }
  wait_vm<0>();
  publish(c, OFF_FY + (w * 32 + j) * 8, 1);
  bar();
}

constexpr int I_Y = 0, I_O = 16384;
DI void phase_irow(const Ctx& c, const char* __restrict__ x) {
  const int lane = c.lane, i = c.w, u = c.u, lo = c.lo, r = lane & 31, h = lane >> 5, sp = 32 * (4 * u + i) + lo;
  u32x2 xr[2][4];
#pragma unroll
  for (int s = 0; s < 2; ++s)
#pragma unroll
    for (int g = 0; g < 4; ++g) xr[s][g] = *(const u32x2*)(x + (size_t)(sp + 1024 * s) * 2048 + r * 64 + h * 8 + g * 16);
  wait_flags(c, OFF_FY + ((u >> 1) * 32 + r) * 8, true, 1);
  {
    const int vy = (lane >> 3) * 32768 + (((lane & 7) ^ ((lo >> 1) & 7)) << 4);
#pragma unroll
    for (int m = 0; m < 4; ++m) { setm0(c.Lb + I_Y + i * 4096 + m * 1024); dma_s<0>(vy, c.rws, OFF_YB + (32 * i + 8 * m) * 32768 + (u * 32 + lo) * 128); }
  }
  const unsigned y5a = __builtin_amdgcn_raw_buffer_load_b32(c.rws, 0, OFF_Y5 + (u * 32 + lo) * 16 + i * 4, SC1);
  const unsigned y5b = __builtin_amdgcn_raw_buffer_load_b32(c.rws, 0, OFF_Y5 + 4096 + (u * 32 + lo) * 16 + i * 4, SC1);
  u32x4 MR[4], MI[2]; float Tc[16], Ts[16];
#pragma unroll
  for (int rho = 0; rho < 16; ++rho) {
    const int e = (rho & 3) + 8 * (rho >> 2) + 4 * h;
    const float t = (float)((e * r) & 31) * (1.f / 32), cs = cos_rev(t), sn = sin_rev(t);
    MR[rho >> 2][rho & 3] = pk_f16(cs, -sn);
    if (rho < 8) MI[rho >> 2][rho & 3] = pk_f16(sn, cs);
    const float tt = (float)(e * r) * (1.f / 1024);
    Tc[rho] = cos_rev(tt) * (1.f / 32); Ts[rho] = sin_rev(tt) * (1.f / 32);
  }
  const float fo = (float)sp * (1.f / 2048), fc = cos_rev(fo), fs = sin_rev(fo);
  wait_vm<0>(); bar();
  u32x4 D[2][2];
#pragma unroll
  for (int rho = 0; rho < 8; ++rho) {
    const int k = r + 32 * ((rho & 3) + 8 * (rho >> 2) + 4 * h);
    const unsigned ya = *(lds_u32)(c.L + I_Y + k * 16 + i * 4), yb = *(lds_u32)(c.L + I_Y + 8192 + k * 16 + i * 4);
    const float br = h_lo(yb) * fc - h_hi(yb) * fs, bi = h_hi(yb) * fc + h_lo(yb) * fs;
    D[0][rho >> 2][rho & 3] = pk_f16(h_lo(ya) + br, h_hi(ya) + bi);
    D[1][rho >> 2][rho & 3] = pk_f16(h_lo(ya) - br, h_hi(ya) - bi);
  }
  const float nb = h_lo(y5b) * fc - h_hi(y5b) * fs;
#pragma unroll
  for (int s = 0; s < 2; ++s) {
    f32x16 Vr = zero16(), Vi = zero16();
#pragma unroll
    for (int k = 0; k < 2; ++k) { Vr = mfma_h(D[s][k], MR[k], Vr); Vi = mfma_h(D[s][k], MI[k], Vi); }
    f32x16 y = zero16();
#pragma unroll
    for (int g = 0; g < 4; ++g) {
      u32x4 a;
#pragma unroll
      for (int ii = 0; ii < 4; ++ii) {
        const int rho = 4 * g + ii;
        a[ii] = pk_f16(Vr[rho] * Tc[rho] - Vi[rho] * Ts[rho], Vi[rho] * Tc[rho] + Vr[rho] * Ts[rho]);
      }
      y = mfma_h(a, MR[g], y);
    }
    const float nyq = (h_lo(y5a) + (s ? -nb : nb)) * (1.f / 32);
#pragma unroll
    for (int g = 0; g < 4; ++g) {
      const int pc = 4 * r + g;
      const u32x2 o = {pk_bf16(bf_lo(xr[s][g][0]) + (y[4 * g] + nyq), bf_hi(xr[s][g][0]) + (y[4 * g + 1] - nyq)),
                       pk_bf16(bf_lo(xr[s][g][1]) + (y[4 * g + 2] + nyq), bf_hi(xr[s][g][1]) + (y[4 * g + 3] - nyq))};
      *(lds_u32x2)(c.L + I_O + pc * 128 + (((2 * i + s) ^ ((pc >> 1) & 7)) << 4) + 8 * h) = o;
    }
  }
  bar();
  copy_out_rows(c, I_O, OFF_YX);
  wait_vm<0>();
  publish(c, OFF_FA + (i * 256 + c.blkp) * 8, 3);
  bar();
}

template <int G> DI void load_stationary(const Ctx& c, u32x4 (&A)[64], rsrc_t rw) {
  constexpr int NST = G == 1 ? 16 : 14, RS = G == 1 ? 7168 : 2048;
  const int lane = c.lane, w = c.w, q = lane >> 4;
  const int vo = (G == 1 ? (64 * (q >> 1) + (q & 1) + 2 * w) : (q + 16 * w)) * RS + (((lane & 15) ^ (q << 2)) << 4);
  const int q2 = (lane & 15) >> 2;
  const lds_char p = c.L + (8 * (lane >> 4) + q2) * 256 + ((w ^ q2) << 6) + 16 * (lane & 3);
#pragma unroll
  for (int half = 0; half < 2; ++half) {
#pragma unroll
    for (int s = 0; s < 8; ++s) {
      const int st = 8 * half + s;
      if (st < NST) {
#pragma unroll
        for (int d = 0; d < 4; ++d) {
          setm0(c.Lb + s * 16384 + w * 4096 + d * 1024);
          dma_w(vo, rw, (G == 1 ? 256 * (st >> 2) + 8 * (st & 3) + 32 * (d >> 1) + 128 * (d & 1) : 64 * st + 4 * d) * RS);
        }
      }
    }
    wait_vm<0>(); bar();
#pragma unroll
    for (int s = 0; s < 8; ++s) {
      const int st = 8 * half + s;
      if (st < NST) {
#pragma unroll
        for (int f = 0; f < 4; ++f) {
          const int off = s * 16384 + (f >> 1) * 8192 + (f & 1) * 8;
          const s16x4 a = __builtin_amdgcn_ds_read_tr16_b64_v4i16((lds_s16x4)(p + off));
          const s16x4 b = __builtin_amdgcn_ds_read_tr16_b64_v4i16((lds_s16x4)(p + off + 1024));
          A[4 * st + f] = __builtin_bit_cast(u32x4, __builtin_shufflevector(a, b, 0, 1, 2, 3, 4, 5, 6, 7));
        }
      }
    }
#pragma unroll
    for (int f = 32 * half; f < 32 * half + 32; ++f) if (f < 4 * NST) asm volatile("" : "+a"(A[f]));
    bar();
  }
}

constexpr int PD = 4;
struct Gm {
  f32x4 acc[4], pend[4], bias[2];
  float e[4];
  unsigned pk[4];
  u32x4 b[PD];
  lds_char p[2];
  int vl, src0, dst0, fl;
};
template <int G> DI int gm_cc(const Ctx& c, int t) { return (2 * c.kq + 2 + t) & 7; }
template <int G, int D> DI void gm_dma(const Ctx& c, const Gm& m, int t) {
  constexpr int Q = G == 1 ? 16 : 14;
  if constexpr ((D & 3) == 0) setm0(c.Lb + (t & 1) * 65536 + c.w * Q * 1024 + D * 1024);
  dma_s<(D & 3) * 1024>(m.vl, c.rws, m.src0 + gm_cc<G>(c, t) * (G == 1 ? 65536 : 229376) + (D & ~3) * 1024);
}
template <int G, int T, int U> DI void gm_unit(const Ctx& c, Gm& m, u32x4 (&A)[64]) {
  constexpr int NU = G == 1 ? 64 : 56, Q = NU / 4, GU = T * NU + U, ks = U >> 1, rt = U & 1;
  constexpr bool EPI = T >= 1 && (G == 1 || T <= 6);
  if constexpr (U == 0 && T >= 1) { for (int j = 0; j < 4; ++j) m.pend[j] = m.acc[j]; }
  const u32x4 b = m.b[GU % PD];
  auto epi = [&](auto PH_) {
    constexpr int ph = decltype(PH_)::value;
    if constexpr (EPI && G == 1) {
      static_for<16>([&](auto V_) {
        constexpr int v = decltype(V_)::value, t = 2 * U + ph - (4 + (v * 98) / 15);
        if constexpr (t >= 0 && (t & 1) == 0) {
          constexpr int k = t / 2;
          float& x = ((float*)&m.pend[v >> 2])[v & 3];
          if constexpr (k == 0) { m.e[v & 3] = __builtin_amdgcn_exp2f(x); PIN(m.e[v & 3]); }
          if constexpr (k == 1) { m.e[v & 3] += 1.f; PIN(m.e[v & 3]); }
          if constexpr (k == 2) { m.e[v & 3] = m.e[v & 3] * m.e[v & 3] + 1.f; PIN(m.e[v & 3]); }
          if constexpr (k == 3) { m.e[v & 3] = __builtin_amdgcn_rcpf(m.e[v & 3]); PIN(m.e[v & 3]); }
          if constexpr (k == 4) { m.e[v & 3] = m.e[v & 3] * -1.38629436f + 0.69314718f; PIN(m.e[v & 3]); }
          if constexpr (k == 5) { x *= m.e[v & 3]; PIN(x); }
          if constexpr (k == 6 && (v & 1)) { m.pk[(v >> 1) & 3] = pk_bf16(m.pend[v >> 2][(v & 3) - 1], x); PIN(m.pk[(v >> 1) & 3]); }
          if constexpr (k == 7 && (v & 7) == 7) st16(u32x4{m.pk[0], m.pk[1], m.pk[2], m.pk[3]}, c.rws, m.vl, m.dst0 + gm_cc<G>(c, T - 1) * 229376 + (v >> 3) * 1024);
        }
      });
    }
    if constexpr (EPI && G == 2 && ph == 1 && U >= 3 && U < 13) {
      constexpr int k = U - 3, hf = k / 5, j = k % 5;
      if constexpr (j < 4) { m.pk[j] = pk_bf16(m.pend[2 * hf + (j >> 1)][2 * (j & 1)], m.pend[2 * hf + (j >> 1)][2 * (j & 1) + 1]); PIN(m.pk[j]); }
      else st16(u32x4{m.pk[0], m.pk[1], m.pk[2], m.pk[3]}, c.rws, m.vl, m.dst0 + gm_cc<G>(c, T - 1) * 32768 + hf * 1024);
    }
  };
  if constexpr (U >= 2 && rt == 0) mf16w(m.acc[0], A[2 * ks], b, m.b[(GU + 1) % PD]); else if constexpr (U >= 2) mf16(m.acc[2 * rt], A[2 * ks], b); else if constexpr (G == 1) mf16c(m.acc[2 * rt], A[2 * ks], b, m.bias[0]); else mf16z(m.acc[2 * rt], A[2 * ks], b);
  GAP();
  u32x4 nb = b;
  if constexpr (U + PD < NU) nb = *(lds_u32x4)(m.p[T & 1] + (U + PD) * 1024);
  else if constexpr (T < 7) nb = *(lds_u32x4)(m.p[(T + 1) & 1] + (U + PD - NU) * 1024);
  epi(std::integral_constant<int, 0>{});
  GAP();
  if constexpr (U >= 2) mf16(m.acc[2 * rt + 1], A[2 * ks + 1], b); else if constexpr (G == 1) mf16c(m.acc[2 * rt + 1], A[2 * ks + 1], b, m.bias[1]); else mf16z(m.acc[2 * rt + 1], A[2 * ks + 1], b);
  m.b[GU % PD] = nb;
  GAP();
  if constexpr (T >= 1 && T < 7 && (U & 1) == 0 && U / 2 < Q) gm_dma<G, U / 2>(c, m, T + 1);
  epi(std::integral_constant<int, 1>{});
  if constexpr (U == NU - PD - 1 && T < 7) {
    GAP();
    if constexpr (G == 1 && T >= 1) wait_vm<1>(); else wait_vm<0>();
    bar_nowait();
    if constexpr (G == 1 && T >= 2) publish(c, m.fl, T - 1);
    if constexpr (G == 2 && T >= 1) publish(c, m.fl, T);
  }
  GAP();
}
template <int G> DI void gm_run(const Ctx& c, Gm& m, u32x4 (&A)[64]) {
  constexpr int NU = G == 1 ? 64 : 56;
  static_for<NU / 4>([&](auto D_) { gm_dma<G, decltype(D_)::value>(c, m, 0); });
  static_for<NU / 4>([&](auto D_) { gm_dma<G, decltype(D_)::value>(c, m, 1); });
  wait_vm<NU / 4>(); bar_nowait();
#pragma unroll
  for (int j = 0; j < PD; ++j) m.b[j] = *(lds_u32x4)(m.p[0] + j * 1024);
  GAP();
  static_for<8>([&](auto T_) { static_for<NU>([&](auto U_) { gm_unit<G, decltype(T_)::value, decltype(U_)::value>(c, m, A); }); });
  asm volatile("s_nop 7\n\ts_nop 7" : "+v"(m.acc[0]), "+v"(m.acc[1]), "+v"(m.acc[2]), "+v"(m.acc[3]));
}
DI float mish2(float t) {
  const float a = __builtin_amdgcn_exp2f(t) + 1.f;
  return t * (__builtin_amdgcn_rcpf(a * a + 1.f) * -1.38629436f + 0.69314718f);
}

__global__ __launch_bounds__(256, 1) void k_fused(const char* __restrict__ x, const char* __restrict__ wr, const char* __restrict__ wi, const char* __restrict__ w1,
                                                  const char* __restrict__ b1, const char* __restrict__ w2, char* __restrict__ out, char* __restrict__ ws) {
  extern __shared__ __attribute__((aligned(16))) char smem[];
  STAMP_DECL; STAMP(0);
  Ctx c;
  c.ws = ws; c.rws = mk_rsrc(ws, WS_USED); c.L = (lds_char)smem; c.Lb = (unsigned)(size_t)c.L;
  c.lane = threadIdx.x & 63; c.w = rfl(threadIdx.x >> 6);
  c.u = blockIdx.x & 7; c.lo = blockIdx.x >> 3; c.blkp = 128 * (c.u & 1) + 32 * (c.u >> 1) + c.lo;
  c.kq = c.u >> 1; c.mt = (c.u & 1) * 4 + (c.lo & 3);
  const int lane = c.lane, w = c.w, n2 = c.lo >> 2;
  phase_front(c, (const unsigned*)x, (const unsigned*)wr, (const unsigned*)wi);
  STAMP(1);
  u32x4 A[64];
  Gm m;
  m.vl = lane * 16; m.p[0] = c.L + lane * 16; m.p[1] = c.L + 65536 + lane * 16;
  if (n2 < 7) {
    const int n1 = c.kq * 7 + n2;
    load_stationary<1>(c, A, mk_rsrc(w1 + n1 * 256, 1024 * 7168 - n1 * 256));
    const u32x4 bv = *(const u32x4*)(b1 + (n1 * 128 + 32 * w + 8 * (lane >> 4)) * 2);
    m.bias[0] = f32x4{bf_lo(bv[0]), bf_hi(bv[0]), bf_lo(bv[1]), bf_hi(bv[1])} * 1.44269504f;
    m.bias[1] = f32x4{bf_lo(bv[2]), bf_hi(bv[2]), bf_lo(bv[3]), bf_hi(bv[3])} * 1.44269504f;
    PIN(m.bias[0]); PIN(m.bias[1]);
    wait_flags(c, OFF_FA + (w * 256 + c.mt * 32 + (lane & 31)) * 8, true, 1);
    STAMP(2);
    m.src0 = OFF_XN + c.mt * 524288 + w * 16384;
    m.dst0 = OFF_H + c.mt * 8 * 229376 + (n1 * 4 + w) * 2048;
    m.fl = OFF_FH + ((c.mt * 4 + c.kq) * 32 + n2 * 4 + w) * 8;
    gm_run<1>(c, m, A);
#pragma unroll
    for (int rt = 0; rt < 2; ++rt)
      st16(u32x4{pk_bf16(mish2(m.acc[2 * rt][0]), mish2(m.acc[2 * rt][1])), pk_bf16(mish2(m.acc[2 * rt][2]), mish2(m.acc[2 * rt][3])),
                 pk_bf16(mish2(m.acc[2 * rt + 1][0]), mish2(m.acc[2 * rt + 1][1])), pk_bf16(mish2(m.acc[2 * rt + 1][2]), mish2(m.acc[2 * rt + 1][3]))},
           c.rws, m.vl, m.dst0 + gm_cc<1>(c, 7) * 229376 + rt * 1024);
    wait_vm<0>();
    publish(c, m.fl, 8);
    bar();
  } else {
    STAMP(2);
    phase_cols(c, c.u * 4 + (c.lo & 3));
  }
  STAMP(3);
  phase_irow(c, x);
  STAMP(4);
  load_stationary<2>(c, A, mk_rsrc(w2 + (size_t)c.kq * 896 * 2048 + n2 * 256, 896 * 2048 - n2 * 256));
  wait_flags(c, OFF_FH + ((c.mt * 4 + c.kq) * 32 + 7 * w + (lane & 7)) * 8, (lane & 7) < 7, 8);
  STAMP(5);
  m.src0 = OFF_H + c.mt * 8 * 229376 + c.kq * 57344 + w * 14336;
  m.dst0 = OFF_PT + (c.mt * 8 + n2) * 262144 + c.kq * 8192 + w * 2048;
  m.fl = OFF_FP + (((c.mt * 8 + n2) * 4 + w) * 4 + c.kq) * 8;
  gm_run<2>(c, m, A);
  STAMP(6);
  {
    const int src = lane & 3;
    const unsigned fo = lane < 4 ? OFF_FP + (((c.mt * 8 + n2) * 4 + w) * 4 + src) * 8 : OFF_FA + ((n2 >> 1) * 256 + c.mt * 32 + c.kq * 8 + (lane & 7)) * 8;
    wait_flags(c, fo, lane < 4 ? src != c.kq : lane < 16, lane < 4 ? 2 * ((c.kq - src - 1) & 3) + 2 : 3);
#pragma unroll
    for (int e = 0; e < 2; ++e)
#pragma unroll
      for (int rt = 0; rt < 2; ++rt) {
        const int cc = 2 * c.kq + e;
        f32x4 a0 = e ? m.acc[2 * rt] : m.pend[2 * rt], a1 = e ? m.acc[2 * rt + 1] : m.pend[2 * rt + 1];
#pragma unroll
        for (int s = 1; s < 4; ++s) {
          const u32x4 t = ld16s(c.rws, m.vl, OFF_PT + (c.mt * 8 + n2) * 262144 + cc * 32768 + ((c.kq + s) & 3) * 8192 + w * 2048 + rt * 1024);
          a0 += f32x4{bf_lo(t[0]), bf_hi(t[0]), bf_lo(t[1]), bf_hi(t[1])}; a1 += f32x4{bf_lo(t[2]), bf_hi(t[2]), bf_lo(t[3]), bf_hi(t[3])};
        }
        const u32x4 t = ld16s(c.rws, m.vl, OFF_YX + (c.mt * 8 + cc) * 65536 + (n2 * 4 + w) * 2048 + rt * 1024);
        a0 += f32x4{bf_lo(t[0]), bf_hi(t[0]), bf_lo(t[1]), bf_hi(t[1])}; a1 += f32x4{bf_lo(t[2]), bf_hi(t[2]), bf_lo(t[3]), bf_hi(t[3])};
        const int R = c.mt * 256 + cc * 32 + rt * 16 + (lane & 15), bp = R >> 3;
        const int uu = 2 * ((bp >> 5) & 3) + (bp >> 7), srow = 32 * (4 * uu + ((R >> 1) & 3)) + (bp & 31) + 1024 * (R & 1);
        *(u32x4*)(out + (size_t)srow * 2048 + n2 * 256 + w * 64 + (lane >> 4) * 16) = u32x4{pk_bf16(a0[0], a0[1]), pk_bf16(a0[2], a0[3]), pk_bf16(a1[0], a1[1]), pk_bf16(a1[2], a1[3])};
      }
  }
  STAMP(7); STAMP_FLUSH(ws, 0);
}

extern "C" void kernel_launch(void* const* d_in, const int* in_sizes, int n_in, void* d_out, int out_size, void* d_ws, size_t ws_size, hipStream_t stream) {
  (void)in_sizes; (void)n_in; (void)out_size; (void)ws_size;
  (void)hipFuncSetAttribute((const void*)k_fused, hipFuncAttributeMaxDynamicSharedMemorySize, 163840);
  k_fused<<<256, 256, 163840, stream>>>((const char*)d_in[0], (const char*)d_in[1], (const char*)d_in[2], (const char*)d_in[4], (const char*)d_in[5], (const char*)d_in[6],
                                         (char*)d_out, (char*)d_ws);
}
```

```cpp
#include <hip/hip_runtime.h>
#include <stdint.h>

typedef float    f32x2  __attribute__((ext_vector_type(2)));
typedef float    f32x4  __attribute__((ext_vector_type(4)));
typedef float    f32x16 __attribute__((ext_vector_type(16)));
typedef _Float16 f16x2  __attribute__((ext_vector_type(2)));
typedef _Float16 f16x8  __attribute__((ext_vector_type(8)));
typedef __bf16   bf16x2 __attribute__((ext_vector_type(2)));
typedef __bf16   bf16x8 __attribute__((ext_vector_type(8)));
typedef short    s16x4  __attribute__((ext_vector_type(4)));
typedef short    s16x8  __attribute__((ext_vector_type(8)));
typedef unsigned u32x2  __attribute__((ext_vector_type(2)));
typedef unsigned u32x4  __attribute__((ext_vector_type(4)));
typedef __attribute__((address_space(3))) s16x4* lds_s16x4;
typedef __attribute__((address_space(3))) char*  lds_char;
typedef __amdgpu_buffer_rsrc_t rsrc_t;
#define DI __device__ __forceinline__
#define GAP() __builtin_amdgcn_sched_barrier(0)

#include <utility>
#include <type_traits>
constexpr unsigned OFF_XN = 0;
constexpr unsigned OFF_X1 = OFF_XN + (4u << 20);
constexpr unsigned OFF_WT = OFF_X1 + (4u << 20);
constexpr unsigned OFF_YB = OFF_WT + (4u << 20);
constexpr unsigned OFF_YX = OFF_YB + (4u << 20);
constexpr unsigned OFF_X5 = OFF_YX + (4u << 20);
constexpr unsigned OFF_W5 = OFF_X5 + 8192;
constexpr unsigned OFF_Y5 = OFF_W5 + 8192;
constexpr unsigned OFF_EP = OFF_Y5 + 8192;
constexpr unsigned OFF_FA = OFF_EP + 8192;
constexpr unsigned OFF_FY = OFF_FA + 8192;
constexpr unsigned OFF_FH = OFF_FY + 8192;
constexpr unsigned OFF_FP = OFF_FH + 8192;
constexpr unsigned OFF_H  = OFF_FP + 8192;
constexpr unsigned H_BYTES = 64u * 224 * 1024;
constexpr unsigned OFF_PT = OFF_H + H_BYTES;
constexpr unsigned OFF_ST = OFF_PT + (16u << 20);
constexpr unsigned WS_USED = OFF_ST;
constexpr int SC1 = 16;
#define ROT 1
#define STAMP_DECL do {} while (0)
#define STAMP(slot) do {} while (0)
#define STAMP_FLUSH(ws, k) do {} while (0)
#define STAMPC(slot) do {} while (0)
#define STAMP_ARG
#define HSTAMP(c, i) do {} while (0)
#define HSTAMP_FLUSH(c) do {} while (0)
#define STAMP_PASS

DI unsigned pk_f16(float a, float b) { f32x2 v = {a, b}; return __builtin_bit_cast(unsigned, __builtin_convertvector(v, f16x2)); }
DI unsigned pk_bf16(float a, float b) { f32x2 v = {a, b}; return __builtin_bit_cast(unsigned, __builtin_convertvector(v, bf16x2)); }
DI float bf_lo(unsigned u) { return __uint_as_float(u << 16); }
DI float bf_hi(unsigned u) { return __uint_as_float(u & 0xffff0000u); }
DI float bf_round(float a) { return bf_lo(pk_bf16(a, a)); }
DI float h_lo(unsigned u) { return (float)__builtin_bit_cast(f16x2, u)[0]; }
DI float h_hi(unsigned u) { return (float)__builtin_bit_cast(f16x2, u)[1]; }
DI f32x16 mfma_h(u32x4 a, u32x4 b, f32x16 c) { return __builtin_amdgcn_mfma_f32_32x32x16_f16(__builtin_bit_cast(f16x8, a), __builtin_bit_cast(f16x8, b), c, 0, 0, 0); }
DI f32x16 mfma_b(u32x4 a, u32x4 b, f32x16 c) {
  return __builtin_amdgcn_mfma_f32_32x32x16_bf16(__builtin_bit_cast(bf16x8, a), __builtin_bit_cast(bf16x8, b), c, 0, 0, 0); }
DI float sin_rev(float r) { return __builtin_amdgcn_sinf(r); }
DI float cos_rev(float r) { return __builtin_amdgcn_cosf(r); }
DI int rfl(int x) { return __builtin_amdgcn_readfirstlane(x); }
DI rsrc_t mk_rsrc(const void* p, unsigned bytes) { return __builtin_amdgcn_make_buffer_rsrc((void*)p, 0, (int)bytes, 0x00020000); }
DI f32x16 zero16() { f32x16 z; for (int i = 0; i < 16; ++i) z[i] = 0.f; return z; }

template <int CTRL> DI float dpp_add(float v) {
  return v + __int_as_float(__builtin_amdgcn_update_dpp(0, __float_as_int(v), CTRL, 0xf, 0xf, true));
}
DI float red_row_h(float v) {
  v = dpp_add<0x128>(v); v = dpp_add<0x124>(v); v = dpp_add<0x122>(v); v = dpp_add<0x121>(v);
  auto r = __builtin_amdgcn_permlane32_swap(__float_as_uint(v), __float_as_uint(v), false, false);
  return __uint_as_float(r[0]) + __uint_as_float(r[1]);
}

template <class F, int... I> DI void static_for_impl(F&& f, std::integer_sequence<int, I...>) { (f(std::integral_constant<int, I>{}), ...); }
template <int N, class F> DI void static_for(F&& f) { static_for_impl(f, std::make_integer_sequence<int, N>{}); }
#define PIN(x) asm volatile("" : "+v"(x))
typedef __attribute__((address_space(3))) u32x4* lds_u32x4;
typedef __attribute__((address_space(3))) u32x2* lds_u32x2;
typedef __attribute__((address_space(3))) unsigned* lds_u32;
typedef __attribute__((address_space(1))) unsigned long long gu64;

DI void setm0(unsigned v) { asm volatile("s_mov_b32 m0, %0\n\ts_nop 0" :: "s"(v) : "memory"); }
template <int OFF> DI void dma_s(int voff, rsrc_t rs, int soff) {
  asm volatile("buffer_load_dwordx4 %0, %1, %2 offen offset:%3 sc1 lds" :: "v"(voff), "s"(rs), "s"(soff), "i"(OFF) : "memory");
}
#define POL_W ""
#define POL_X ""
template <int OFF> DI void dma_w(int voff, rsrc_t rs, int soff) {
  asm volatile("buffer_load_dwordx4 %0, %1, %2 offen offset:%3 " POL_W " lds" :: "v"(voff), "s"(rs), "s"(soff), "i"(OFF) : "memory");
}
template <int OFF> DI void ldx1(unsigned& d, int voff, rsrc_t rs) { asm volatile("buffer_load_dword %0, %1, %2, 0 offen offset:%3 " POL_X : "=v"(d) : "v"(voff), "s"(rs), "i"(OFF) : "memory"); }
template <int OFF> DI void ldx4(u32x4& d, int voff, rsrc_t rs) { asm volatile("buffer_load_dwordx4 %0, %1, %2, 0 offen offset:%3 " POL_X : "=v"(d) : "v"(voff), "s"(rs), "i"(OFF) : "memory"); }
DI void ldf(u32x2& d, int voff, rsrc_t rs) { asm volatile("buffer_load_dwordx2 %0, %1, %2, 0 offen sc1" : "=v"(d) : "v"(voff), "s"(rs) : "memory"); }
DI void mh(f32x16& acc, u32x4 a, u32x4 b) { asm volatile("v_mfma_f32_32x32x16_f16 %0, %1, %2, %0" : "+v"(acc) : "v"(a), "v"(b)); }
DI void mhz(f32x16& acc, u32x4 a, u32x4 b) { asm volatile("v_mfma_f32_32x32x16_f16 %0, %1, %2, 0" : "=&v"(acc) : "v"(a), "v"(b)); }
DI void mhn(f32x16& acc, u32x4 a, u32x4 b) { asm volatile("s_nop 1\n\tv_mfma_f32_32x32x16_f16 %0, %1, %2, %0" : "+v"(acc) : "v"(a), "v"(b)); }
DI void mhnz(f32x16& acc, u32x4 a, u32x4 b) { asm volatile("s_nop 1\n\tv_mfma_f32_32x32x16_f16 %0, %1, %2, 0" : "=&v"(acc) : "v"(a), "v"(b)); }
DI void mh_done(f32x16& a, f32x16& b) { asm volatile("s_nop 15" : "+v"(a), "+v"(b)); }
DI f32x2 pr2(const f32x16& v, int i) { return f32x2{v[i], v[i + 1]}; }
template <int N> DI void wait_vm() { asm volatile("s_waitcnt vmcnt(%0)" :: "i"(N) : "memory"); }
DI void bar() { asm volatile("s_waitcnt lgkmcnt(0)\n\ts_barrier" ::: "memory"); }
DI void bar_nowait() { asm volatile("s_barrier" ::: "memory"); }
DI void st16(u32x4 v, rsrc_t rs, int voff, int soff) { __builtin_amdgcn_raw_buffer_store_b128(v, rs, voff, soff, SC1); }
DI u32x4 ld16s(rsrc_t rs, int voff, int soff) { return __builtin_amdgcn_raw_buffer_load_b128(rs, voff, soff, SC1); }
DI void mf16w(f32x4& acc, u32x4 a, u32x4 b, u32x4 other) { asm volatile("v_mfma_f32_16x16x32_bf16 %0, %1, %2, %0" : "+v"(acc) : "a"(a), "v"(b), "v"(other)); }
DI void mf16(f32x4& acc, u32x4 a, u32x4 b) { asm volatile("v_mfma_f32_16x16x32_bf16 %0, %1, %2, %0" : "+v"(acc) : "a"(a), "v"(b)); }
DI void mf16z(f32x4& acc, u32x4 a, u32x4 b) { asm volatile("v_mfma_f32_16x16x32_bf16 %0, %1, %2, 0" : "=&v"(acc) : "a"(a), "v"(b)); }
DI void mf16c(f32x4& acc, u32x4 a, u32x4 b, f32x4 c0) { asm volatile("v_mfma_f32_16x16x32_bf16 %0, %1, %2, %3" : "=&v"(acc) : "a"(a), "v"(b), "v"(c0)); }

DI void mf16v(f32x4& acc, u32x4 a, u32x4 b) { asm volatile("v_mfma_f32_16x16x32_bf16 %0, %1, %2, %0" : "+v"(acc) : "v"(a), "v"(b)); }
DI void mf16vw(f32x4& acc, u32x4 a, u32x4 b, u32x4 other) { asm volatile("v_mfma_f32_16x16x32_bf16 %0, %1, %2, %0" : "+v"(acc) : "v"(a), "v"(b), "v"(other)); }
DI void mf16vz(f32x4& acc, u32x4 a, u32x4 b) { asm volatile("v_mfma_f32_16x16x32_bf16 %0, %1, %2, 0" : "=&v"(acc) : "v"(a), "v"(b)); }
template <int IMM> DI void m0q(unsigned base) { asm volatile("s_add_u32 m0, %0, %1" :: "s"(base), "i"(IMM) : "memory", "scc"); }
template <int IMM> DI void m0n(unsigned base) { asm volatile("s_add_u32 m0, %0, %1\n\ts_nop 0" :: "s"(base), "i"(IMM) : "memory", "scc"); }
DI void lds1(unsigned& d, int voff, rsrc_t rs, int soff) { asm volatile("buffer_load_dword %0, %1, %2, %3 offen sc1" : "=v"(d) : "v"(voff), "s"(rs), "s"(soff) : "memory"); }
DI void lds4(u32x4& d, int voff, rsrc_t rs, int soff) { asm volatile("buffer_load_dwordx4 %0, %1, %2, %3 offen sc1" : "=v"(d) : "v"(voff), "s"(rs), "s"(soff) : "memory"); }
template <int OFF> DI void ldx2(u32x2& d, int voff, rsrc_t rs, int soff) { asm volatile("buffer_load_dwordx2 %0, %1, %2, %3 offen offset:%4" : "=v"(d) : "v"(voff), "s"(rs), "s"(soff), "i"(OFF) : "memory"); }
DI u32x4 a2v(u32x4 a) {
  u32x4 r;
#pragma unroll
  for (int i = 0; i < 4; ++i) { unsigned t; asm volatile("v_accvgpr_read_b32 %0, %1" : "=v"(t) : "a"(a[i])); r[i] = t; }
  return r;
}
DI u32x4 a2a(u32x4 a) {
  u32x4 r;
#pragma unroll
  for (int i = 0; i < 4; ++i) { unsigned t; asm volatile("v_accvgpr_mov_b32 %0, %1" : "=a"(t) : "a"(a[i])); r[i] = t; }
  return r;
}
DI unsigned v2a(unsigned v) { unsigned t; asm volatile("v_accvgpr_write_b32 %0, %1" : "=a"(t) : "v"(v)); return t; }
DI lds_char lxor(lds_char p, unsigned x) { return (lds_char)(uintptr_t)(((unsigned)(uintptr_t)p) ^ x); }
DI f16x2 H2(unsigned u) { return __builtin_bit_cast(f16x2, u); }
DI unsigned U1(f16x2 h) { return __builtin_bit_cast(unsigned, h); }
DI unsigned cm_a(unsigned z, unsigned t) { const f16x2 a = H2(z), b = H2(t); return U1(f16x2{a[1], a[0]} * f16x2{b[1], -b[1]}); }
DI unsigned cm_b(unsigned z, unsigned t, unsigned p) { const f16x2 b = H2(t); return U1(__builtin_elementwise_fma(H2(z), f16x2{b[0], b[0]}, H2(p))); }
DI unsigned cmulc(unsigned z, unsigned t) { return cm_b(z, t, cm_a(z, t)); }
DI unsigned cmuls(unsigned z, unsigned t) {
  const f16x2 a = H2(z), b = H2(t);
  return U1(__builtin_elementwise_fma(f16x2{a[1], a[0]}, f16x2{b[0], b[0]}, a * f16x2{b[1], -b[1]}));
}
template <int SW> DI void cm2(unsigned& o0, unsigned& o1, float r0, float i0, float r1, float i1, unsigned t0, unsigned t1) {
  unsigned p0, p1;
  if constexpr (SW == 0)
    asm volatile("v_cvt_pk_f16_f32 %0, %4, %5\n\tv_cvt_pk_f16_f32 %1, %6, %7\n\t"
                 "v_pk_mul_f16 %2, %0, %8 op_sel:[1,1] op_sel_hi:[0,1] neg_hi:[0,1]\n\tv_pk_mul_f16 %3, %1, %9 op_sel:[1,1] op_sel_hi:[0,1] neg_hi:[0,1]\n\t"
                 "v_pk_fma_f16 %0, %0, %8, %2 op_sel_hi:[1,0,1]\n\tv_pk_fma_f16 %1, %1, %9, %3 op_sel_hi:[1,0,1]"
                 : "=&v"(o0), "=&v"(o1), "=&v"(p0), "=&v"(p1) : "v"(r0), "v"(i0), "v"(r1), "v"(i1), "v"(t0), "v"(t1));
  else
    asm volatile("v_cvt_pk_f16_f32 %0, %4, %5\n\tv_cvt_pk_f16_f32 %1, %6, %7\n\t"
                 "v_pk_mul_f16 %2, %0, %8 op_sel:[0,1] neg_hi:[0,1]\n\tv_pk_mul_f16 %3, %1, %9 op_sel:[0,1] neg_hi:[0,1]\n\t"
                 "v_pk_fma_f16 %0, %0, %8, %2 op_sel:[1,0,0] op_sel_hi:[0,0,1]\n\tv_pk_fma_f16 %1, %1, %9, %3 op_sel:[1,0,0] op_sel_hi:[0,0,1]"
                 : "=&v"(o0), "=&v"(o1), "=&v"(p0), "=&v"(p1) : "v"(r0), "v"(i0), "v"(r1), "v"(i1), "v"(t0), "v"(t1));
}
DI unsigned i_cvt(float a, float b) { unsigned r; asm volatile("v_cvt_pk_f16_f32 %0, %1, %2" : "=v"(r) : "v"(a), "v"(b)); return r; }
DI unsigned i_cma(unsigned z, unsigned t) { unsigned r; asm volatile("v_pk_mul_f16 %0, %1, %2 op_sel:[1,1] op_sel_hi:[0,1] neg_hi:[0,1]" : "=v"(r) : "v"(z), "v"(t)); return r; }
DI unsigned i_cmb(unsigned z, unsigned t, unsigned p) { unsigned r; asm volatile("v_pk_fma_f16 %0, %1, %2, %3 op_sel_hi:[1,0,1]" : "=v"(r) : "v"(z), "v"(t), "v"(p)); return r; }
DI unsigned i_add(unsigned a, unsigned b) { unsigned r; asm volatile("v_pk_add_f16 %0, %1, %2" : "=v"(r) : "v"(a), "v"(b)); return r; }
DI unsigned i_sub(unsigned a, unsigned b) { unsigned r; asm volatile("v_pk_add_f16 %0, %1, %2 neg_lo:[0,1] neg_hi:[0,1]" : "=v"(r) : "v"(a), "v"(b)); return r; }
DI unsigned i_mul(unsigned a, unsigned b) { unsigned r; asm volatile("v_pk_mul_f16 %0, %1, %2" : "=v"(r) : "v"(a), "v"(b)); return r; }
DI unsigned cs_pk(float t) { return pk_f16(cos_rev(t), sin_rev(t)); }
DI unsigned cs_pk32(float t) { return U1(H2(cs_pk(t)) * (_Float16)0.03125f); }

struct Ctx {
  char* ws; rsrc_t rws; lds_char L; unsigned Lb, tlo, thi, wb4, wb7, wb8;
  int lane, w, u, lo, blkp, mt, kq, rot, v0;
};
constexpr int OOB = 0x7ffffff0;
DI void st8(const Ctx& c, unsigned lo, unsigned hi, unsigned off) { __builtin_amdgcn_raw_buffer_store_b64(u32x2{lo, hi}, c.rws, c.v0, off, SC1); }
DI void publish(const Ctx& c, unsigned off, unsigned count) { st8(c, c.tlo, c.thi + count, off); }
DI bool flag_ok(const Ctx& c, u32x2 v, bool active, unsigned need);
template <int SLP = 4> DI void wait_flags(const Ctx& c, unsigned off, bool active, unsigned need) {
#pragma unroll 1
  for (unsigned spins = 0; spins < (1u << 20); ++spins) {
    u32x2 v;
    asm volatile("buffer_load_dwordx2 %0, %1, %2, 0 offen sc1\n\ts_waitcnt vmcnt(0)" : "=&v"(v) : "v"(off), "s"(c.rws) : "memory");
    if (flag_ok(c, v, active, need)) break;
    __builtin_amdgcn_s_sleep(SLP);
  }
}
DI bool flag_ok(const Ctx& c, u32x2 v, bool active, unsigned need) { const unsigned cnt = v[1] - c.thi; return __all(!active || (v[0] == c.tlo && cnt >= need && cnt < 16u)); }
template <int SW> DI int row_swz(int pc) { return SW ? 2 * (pc & 3) : (pc >> 2) & 7; }
template <int SW> DI void copy_out_rows(const Ctx& c, int img, unsigned dst) {
  const int so = dst + (c.blkp >> 2) * 65536 + ((c.blkp >> 1) & 1) * 1024 + (c.blkp & 1) * 128;
#pragma unroll
  for (int m = 0; m < 4; ++m) {
    const int pc = 32 * c.w + 8 * m + (c.lane >> 3), row = c.lane & 7;
    const u32x4 v = *(lds_u32x4)(c.L + img + pc * 128 + ((row ^ row_swz<SW>(pc)) << 4));
    st16(v, c.rws, (pc >> 2) * 2048 + (pc & 3) * 256 + row * 16, so);
  }
}

constexpr int R_STR = 1088 * 4, R_XN = 0, R_XI = 16384, R_WI = R_XI + 4 * R_STR, R_PF = R_WI + 4 * R_STR, R_ST = 65536;
constexpr int L_ST = 98304, L_TB = 147456, L_Y = L_ST, L_O = L_ST + 16384, C_X5 = 131072;

struct Stat { rsrc_t rw; int vo[4], so, step, wrap; lds_char p; };
template <int N, int LDS> DI void stat_dma(const Ctx& c, Stat& s) {
  if constexpr ((N & 3) == 0) m0n<LDS>(c.wb4);
  dma_w<(N & 3) * 1024>(s.vo[N & 3], s.rw, s.so);
  if constexpr ((N & 3) == 3) { s.so += s.step; s.so -= s.so >= s.wrap ? s.wrap : 0; asm volatile("" : "+s"(s.so)); }
}
DI s16x4 trr(lds_char p) { return __builtin_amdgcn_ds_read_tr16_b64_v4i16((lds_s16x4)p); }
DI u32x4 cat(s16x4 a, s16x4 b) { return __builtin_bit_cast(u32x4, __builtin_shufflevector(a, b, 0, 1, 2, 3, 4, 5, 6, 7)); }
DI void stat_pin(u32x4 (&A)[64], int st0, int st1) {
#pragma unroll
  for (int f = 4 * st0; f < 4 * st1; ++f) asm volatile("" : "+a"(A[f]));
}
DI Stat stat_make(const Ctx& c, const char* base, int rs, int rows, int lds) {
  Stat s; const int q = c.lane >> 4, q2 = (c.lane & 15) >> 2;
  s.rw = mk_rsrc(base, (rows - 1) * rs + 256); s.step = 64 * rs; s.wrap = rows * rs; s.so = (16 * c.w + (rows >> 2) * c.rot) * rs;
#pragma unroll
  for (int d = 0; d < 4; ++d) s.vo[d] = (q + 4 * d) * rs - 1024 * d + (((c.lane & 15) ^ (q << 2)) << 4);
  s.p = c.L + lds + (8 * q + q2) * 256 + ((c.w ^ q2) << 6) + 16 * (c.lane & 3);
  return s;
}
template <int N> DI void fq(const Ctx& c, Stat& s) { stat_dma<N, R_ST + ((N >> 2) % 6) * 16384>(c, s); GAP(); }
template <int F> DI void fr(const Stat& s, u32x4 (&A)[64]) {
  constexpr int off = ((F >> 2) % 6) * 16384 + ((F >> 1) & 1) * 8192 + (F & 1) * 8;
  A[F] = cat(trr(s.p + off), trr(s.p + off + 1024));
}
template <int J, int N> DI void fbar(u32x4 (&A)[64]) {
  wait_vm<N>();
  if constexpr (J >= 1) stat_pin(A, 2 * J - 2, 2 * J);
  bar();
}
template <int J> DI void fstream(const Ctx& c, Stat& s, u32x4 (&A)[64]) {
  static_for<8>([&](auto I_) { constexpr int i = decltype(I_)::value; fr<8 * J + i>(s, A); if constexpr (J + 2 < 8) fq<8 * J + 16 + i>(c, s); });
}

struct Spec { u32x4 v[9]; int vo, vo5; unsigned f; };
DI void spec_store(const Ctx& c, const Spec& p, int j) {
  if (j < 8) st16(p.v[j], c.rws, p.vo, ((j & 1) ? OFF_WT : OFF_X1) + (c.u * 32 + c.lo) * 128 + (j >> 1) * 262144);
  else st16(p.v[8], c.rws, p.vo5, 0);
}
struct FrontIn { unsigned d[16]; u32x2 ep; };
DI void front_loads(const Ctx& c, FrontIn& in, const char* __restrict__ x) {
  const int r = c.lane & 31, sp = 32 * (4 * c.u + c.w) + c.lo;
  const rsrc_t rx = mk_rsrc(x + (size_t)sp * 2048, 1025 * 2048);
  const int vx = (r >> 4) * (1024 * 2048) + 64 * (c.lane >> 5) + 4 * (r & 15);
  asm volatile("s_nop 4" ::: "memory");
  static_for<16>([&](auto A_) { constexpr int a = decltype(A_)::value; ldx1<128 * a>(in.d[a], vx, rx); });
  ldf(in.ep, OFF_EP, c.rws);
  GAP();
}
DI void phase_front(Ctx& c, Stat& st, u32x4 (&A)[64], Spec& sp_, FrontIn& in, const char* __restrict__ wr, const char* __restrict__ wi STAMP_ARG) {
  const int lane = c.lane, i = c.w, u = c.u, lo = c.lo, r = lane & 31, h = lane >> 5;
  const int sp = 32 * (4 * u + i) + lo, sg = r >> 4, be = r & 15;
  const float rf = (float)r, hf = (float)h;
  u32x4 Cf[2], Sf[2], T[2][2], G[2][2];
  {
    const float r16 = rf * (1.f / 16), rh = rf * hf * (1.f / 32), rk = rf * (1.f / 1024), rk0 = rk * hf * 8.f;
    static_for<4>([&](auto K_) {
      constexpr int k = decltype(K_)::value >> 1, jb = 2 * (decltype(K_)::value & 1);
      fq<decltype(K_)::value>(c, st);
#pragma unroll
      for (int j2 = jb; j2 < jb + 2; ++j2) {
        const float t0 = r16 * (float)(8 * k + 2 * j2) + rh, t1 = r16 * (float)(8 * k + 2 * j2 + 1) + rh;
        unsigned a = pk_f16(cos_rev(t0), cos_rev(t1)), b = pk_f16(sin_rev(-t0), sin_rev(-t1));
        PIN(a); PIN(b); Cf[k][j2] = a; Sf[k][j2] = b;
      }
    });
    static_for<4>([&](auto K_) {
      constexpr int j = decltype(K_)::value >> 1, g = decltype(K_)::value & 1;
#pragma unroll
      for (int q = 0; q < 4; ++q) T[j][g][q] = cs_pk32(rk * (float)(2 * (q + 8 * g) + j) + rk0);
      PIN(T[j][g]);
      fq<4 + decltype(K_)::value>(c, st);
    });
  }
  STAMP(1);
  wait_vm<9>();
#pragma unroll
  for (int a = 0; a < 16; ++a) PIN(in.d[a]);
  PIN(in.ep);
  STAMP(2);
  f32x2 v[16], s2 = {0.f, 0.f};
#pragma unroll
  for (int a = 0; a < 16; ++a) { v[a] = f32x2{bf_lo(in.d[a]), bf_hi(in.d[a])}; s2 += v[a]; }
  PIN(s2); fq<8>(c, st);
  const float mean = red_row_h(s2[0] + s2[1]) * (1.f / 1024);
  f32x2 q2 = {0.f, 0.f};
#pragma unroll
  for (int a = 0; a < 16; ++a) { const f32x2 t = v[a] - mean; q2 += t * t; }
  PIN(q2); fq<9>(c, st);
  const float var = red_row_h(q2[0] + q2[1]) * (1.f / 1024);
  const float mb = bf_round(mean), sb = bf_round(__builtin_amdgcn_sqrtf(bf_round(bf_round(var) + 1e-5f)));
  float inv = __builtin_amdgcn_rcpf(sb);
  PIN(inv); fq<10>(c, st);
  {
    const lds_char px = c.L + R_XN + (4 * h + (be >> 2)) * 128 + (((2 * i + sg) ^ (2 * (be >> 2))) << 4) + 4 * (be & 3);
    const float invc = inv * 1.44269504f;
#pragma unroll
    for (int a = 0; a < 16; ++a) { v[a] -= mb; const f32x2 t = v[a] * invc; *(lds_u32)(px + a * 1024) = pk_bf16(t[0], t[1]); }
  }
  fq<11>(c, st);
  c.tlo = rfl(in.ep[0] + 1u); c.thi = rfl(in.ep[1]);
  fbar<0, 4>(A);
  copy_out_rows<1>(c, R_XN, OFF_XN);
  STAMP(3);
  static_for<8>([&](auto F_) { fr<decltype(F_)::value>(st, A); });
  GAP();
  f32x2 n2 = {0.f, 0.f};
#pragma unroll
  for (int a = 0; a < 16; ++a) { v[a] *= inv; n2 += v[a]; }
  float nyq = n2[0] - n2[1];
  PIN(nyq);
  f32x16 Ur[2], Ui[2];
  const float bk = (float)be * (1.f / 32), bk0 = bk * hf * 8.f + (float)sg * 0.25f;
  static_for<4>([&](auto K_) {
    constexpr int j = decltype(K_)::value >> 1, k = decltype(K_)::value & 1;
    u32x4 a;
#pragma unroll
    for (int j2 = 0; j2 < 4; ++j2) a[j2] = pk_f16(v[8 * k + 2 * j2][j], v[8 * k + 2 * j2 + 1][j]);
    if constexpr (k == 0) mhnz(Ur[j], a, Cf[0]); else mhn(Ur[j], a, Cf[1]);
    fq<12 + 3 * decltype(K_)::value>(c, st);
    unsigned g0 = cs_pk(bk * (float)(2 * (0 + 8 * k) + j) + bk0), g1 = cs_pk(bk * (float)(2 * (1 + 8 * k) + j) + bk0);
    PIN(g0); PIN(g1);
    if constexpr (k == 0) mhz(Ui[j], a, Sf[0]); else mh(Ui[j], a, Sf[1]);
    fq<13 + 3 * decltype(K_)::value>(c, st);
    unsigned g2 = cs_pk(bk * (float)(2 * (2 + 8 * k) + j) + bk0), g3 = cs_pk(bk * (float)(2 * (3 + 8 * k) + j) + bk0);
    PIN(g2); PIN(g3);
    G[j][k] = u32x4{g0, g1, g2, g3};
    fq<14 + 3 * decltype(K_)::value>(c, st);
  });
  nyq = red_row_h(nyq) * (1.f / 32);
  sp_.f = cs_pk((float)sp * (1.f / 2048)); PIN(sp_.f); PIN(nyq);
  wait_vm<12>();
  publish(c, OFF_FA + (i * 256 + c.blkp) * 8, 1);
  STAMP(4);
  fbar<1, 9>(A);
  STAMP(5);
  PIN(Ur[0]); PIN(Ui[0]); PIN(Ur[1]); PIN(Ui[1]);
  f32x16 X[2];
  static_for<8>([&](auto K_) {
    constexpr int K = decltype(K_)::value, j = K >> 2, g = (K >> 1) & 1, s = K & 1, o = 8 * s + 4 * g;
    unsigned b0, b1, b2, b3;
    fr<8 + K>(st, A);
    cm2<0>(b0, b1, Ur[j][o], Ui[j][o], Ur[j][o + 1], Ui[j][o + 1], T[j][g][0], T[j][g][1]);
    cm2<0>(b2, b3, Ur[j][o + 2], Ui[j][o + 2], Ur[j][o + 3], Ui[j][o + 3], T[j][g][2], T[j][g][3]);
    if constexpr (K < 2) mhnz(X[s], G[j][g], u32x4{b0, b1, b2, b3}); else mhn(X[s], G[j][g], u32x4{b0, b1, b2, b3});
    fq<24 + K>(c, st);
  });
  fbar<2, 8>(A);
  STAMP(6);
  mh_done(X[0], X[1]);
  const lds_u32 xi = (lds_u32)(c.L + R_XI + i * R_STR) + r + 128 * h;
  static_for<4>([&](auto Q_) {
    constexpr int q = decltype(Q_)::value;
    fr<16 + 2 * q>(st, A); fr<17 + 2 * q>(st, A);
    const f32x2 er = pr2(X[0], 2 * q) + pr2(X[1], 2 * q), ei = pr2(X[0], 8 + 2 * q) + pr2(X[1], 8 + 2 * q);
    const f32x2 dr = pr2(X[0], 2 * q) - pr2(X[1], 2 * q), di = pr2(X[0], 8 + 2 * q) - pr2(X[1], 8 + 2 * q);
    constexpr int k0 = 32 * ((2 * q & 3) + 8 * (q >> 1));
    xi[k0] = pk_f16(er[0], ei[0]); xi[k0 + 32] = pk_f16(er[1], ei[1]);
    fq<32 + 2 * q>(c, st);
    unsigned o0, o1;
    cm2<0>(o0, o1, dr[0], di[0], dr[1], di[1], sp_.f, sp_.f);
    xi[513 + k0] = o0; xi[513 + k0 + 32] = o1;
    fq<33 + 2 * q>(c, st);
  });
  {
    const float n0 = __int_as_float(__builtin_amdgcn_readlane(__float_as_int(nyq), 0)), n1 = __int_as_float(__builtin_amdgcn_readlane(__float_as_int(nyq), 16));
    const lds_u32 x0 = (lds_u32)(c.L + R_XI + i * R_STR);
    x0[512] = pk_f16(n0 + n1, 0.f); x0[1025] = cmulc(pk_f16(n0 - n1, 0.f), sp_.f);
  }
  unsigned Rt, It; u32x4 R[2], I[2];
  {
    const rsrc_t rr = mk_rsrc(wr + (size_t)sp * 2052, 2052), ri = mk_rsrc(wi + (size_t)sp * 2052, 2052);
    ldx4<0>(R[0], lane * 16, rr); ldx4<1024>(R[1], lane * 16, rr); ldx4<0>(I[0], lane * 16, ri); ldx4<1024>(I[1], lane * 16, ri);
    ldx1<2048>(Rt, 0, rr); ldx1<2048>(It, 0, ri);
  }
  fbar<3, 14>(A);
  STAMP(7);
  const lds_u32 xa = (lds_u32)(c.L + R_XI) + 256 * i + lane + (i >> 1);
  static_for<8>([&](auto I_) {
    constexpr int k = decltype(I_)::value;
    fr<24 + k>(st, A);
    if constexpr (k & 1) { constexpr int e = 64 * (k >> 1); sp_.v[k - 1] = u32x4{xa[e], xa[1088 + e], xa[2176 + e], xa[3264 + e]}; }
    fq<40 + k>(c, st);
  });
  fbar<4, 8>(A);
  STAMP(8);
  PIN(R[0]); PIN(R[1]); PIN(I[0]); PIN(I[1]); PIN(Rt); PIN(It);
  {
    const lds_char wimg = c.L + R_WI + i * R_STR;
    static_for<4>([&](auto K_) {
      constexpr int k = decltype(K_)::value, m = k >> 1, o = 2 * (k & 1);
      fr<32 + 2 * k>(st, A); fr<33 + 2 * k>(st, A);
      fq<48 + 2 * k>(c, st);
      *(lds_u32x4)(wimg + 2048 * m + 32 * lane + 8 * o) = u32x4{pk_f16(bf_lo(R[m][o]), bf_lo(I[m][o])), pk_f16(bf_hi(R[m][o]), bf_hi(I[m][o])),
                                                                pk_f16(bf_lo(R[m][o + 1]), bf_lo(I[m][o + 1])), pk_f16(bf_hi(R[m][o + 1]), bf_hi(I[m][o + 1]))};
      fq<49 + 2 * k>(c, st);
    });
    *(lds_u32x2)(wimg + 4096) = u32x2{pk_f16(bf_lo(Rt), bf_lo(It)), pk_f16(bf_hi(Rt), bf_hi(It))};
  }
}
DI void front_tail(const Ctx& c, Stat& st, u32x4 (&A)[64], Spec& sp_) {
  const int lane = c.lane, i = c.w;
  const lds_u32 wa = (lds_u32)(c.L + R_WI) + 256 * i + lane + (i >> 1);
  static_for<8>([&](auto I_) {
    constexpr int k = decltype(I_)::value;
    fr<40 + k>(st, A);
    if constexpr (k & 1) { constexpr int e = 64 * (k >> 1); sp_.v[k] = u32x4{wa[e], wa[1088 + e], wa[2176 + e], wa[3264 + e]}; }
    fq<56 + k>(c, st);
  });
  sp_.vo = (32 * i + (lane >> 3)) * 32768 + (((lane & 7) ^ ((c.lo >> 1) & 7)) << 4);
  const lds_u32 x5 = (lds_u32)(c.L + R_XI) + 512 + 513 * (lane & 1) + ((lane & 2) ? 4 * 1088 : 0);
  sp_.v[8] = u32x4{x5[0], x5[1088], x5[2176], x5[3264]};
  sp_.vo5 = (i == 0 && lane < 4) ? OFF_X5 + lane * 4096 + (c.u * 32 + c.lo) * 16 : OOB;
#pragma unroll
  for (int j = 0; j < 9; ++j) PIN(sp_.v[j]);
}
DI void table_build(const Ctx& c) {
  const float rf = (float)(c.lane & 31) * (1.f / 32), e0 = (float)(8 * c.w + 4 * (c.lane >> 5));
  u32x4 a, b, t;
#pragma unroll
  for (int q = 0; q < 4; ++q) {
    const float th = rf * (e0 + (float)q), cs = cos_rev(th), sn = sin_rev(th);
    a[q] = pk_f16(cs, sn); b[q] = pk_f16(-sn, cs); t[q] = cs_pk32(th * (1.f / 32));
  }
  const lds_char p = c.L + L_TB + c.w * 1024 + c.lane * 16;
  *(lds_u32x4)p = a; *(lds_u32x4)(p + 4096) = b; *(lds_u32x4)(p + 8192) = t;
}

struct ColConst { u32x4 MR[4], MI[4], T[4]; };
template <class F> DI void col_pair(const ColConst& k, lds_char pa, lds_char pb, int gs, int wo, F&& side) {
  f32x16 R[4][2], I[4][2];
  u32x4 D[2][4], W[2][4];
  unsigned b[24][4];
#pragma unroll
  for (int g = 0; g < 4; ++g) D[0][g] = *(lds_u32x4)(pa + g * gs);
#pragma unroll
  for (int g = 0; g < 4; ++g) D[1][g] = *(lds_u32x4)(pb + g * gs);
  auto half = [&](auto H_) {
    constexpr int h = decltype(H_)::value, n = h >> 1, q = h & 1, sgm = n >> 2, g = n & 3, p = sgm & 1, st = sgm >> 1, o = 4 * g + 2 * q;
    if constexpr (h < 48) {
      if constexpr (st == 1) cm2<1>(b[n][2 * q], b[n][2 * q + 1], R[1][p][o], I[1][p][o], R[1][p][o + 1], I[1][p][o + 1], W[p][g][2 * q], W[p][g][2 * q + 1]);
      else cm2<0>(b[n][2 * q], b[n][2 * q + 1], R[st][p][o], I[st][p][o], R[st][p][o + 1], I[st][p][o + 1], k.T[g][2 * q], k.T[g][2 * q + 1]);
    }
  };
  static_for<16>([&](auto M_) {
    constexpr int mI = decltype(M_)::value, p = mI >> 3, g = (mI >> 1) & 3;
    if constexpr (mI & 1) { if constexpr (g == 0) mhz(I[0][p], D[p][g], k.MI[g]); else mh(I[0][p], D[p][g], k.MI[g]); }
    else { if constexpr (g == 0) mhz(R[0][p], D[p][g], k.MR[g]); else mh(R[0][p], D[p][g], k.MR[g]); }
    if constexpr (mI < 8) W[mI >> 2][mI & 3] = *(lds_u32x4)((mI < 4 ? pa : pb) + wo + (mI & 3) * gs);
    side(M_);
    if constexpr (mI >= 13) half(std::integral_constant<int, mI - 13>{});
    GAP();
  });
  static_for<48>([&](auto M_) {
    constexpr int mI = decltype(M_)::value, n = mI >> 1, sgm = n >> 2, g = n & 3, p = sgm & 1, st = sgm >> 1;
    const u32x4 bb = {b[n][0], b[n][1], b[n][2], b[n][3]};
    f32x16& acc = (mI & 1) ? I[st + 1][p] : R[st + 1][p];
    const u32x4 mat = (mI & 1) ? k.MI[g] : k.MR[g];
    if constexpr (st == 1) { if constexpr (g == 0) mhz(acc, bb, mat); else mh(acc, bb, mat); } else { if constexpr (g == 0) mhz(acc, mat, bb); else mh(acc, mat, bb); }
    side(std::integral_constant<int, 16 + mI>{});
    half(std::integral_constant<int, mI + 3>{});
    if constexpr (mI >= 44) {
      constexpr int g2 = mI - 44;
      *(lds_u32x4)(pa + g2 * gs) = u32x4{pk_f16(R[3][0][4 * g2], I[3][0][4 * g2]), pk_f16(R[3][0][4 * g2 + 1], I[3][0][4 * g2 + 1]),
                                         pk_f16(R[3][0][4 * g2 + 2], I[3][0][4 * g2 + 2]), pk_f16(R[3][0][4 * g2 + 3], I[3][0][4 * g2 + 3])};
    }
    GAP();
  });
  asm volatile("s_nop 11" : "+v"(R[3][1]), "+v"(I[3][1]));
#pragma unroll
  for (int g = 0; g < 4; ++g)
    *(lds_u32x4)(pb + g * gs) = u32x4{pk_f16(R[3][1][4 * g], I[3][1][4 * g]), pk_f16(R[3][1][4 * g + 1], I[3][1][4 * g + 1]),
                                      pk_f16(R[3][1][4 * g + 2], I[3][1][4 * g + 2]), pk_f16(R[3][1][4 * g + 3], I[3][1][4 * g + 3])};
}
DI void phase_cols(const Ctx& c, int j STAMP_ARG) {
  const int lane = c.lane, w = c.w, r = lane & 31, h = lane >> 5, vl = lane * 16;
  ColConst k;
#pragma unroll
  for (int g = 0; g < 4; ++g) {
    const lds_char p = c.L + L_TB + g * 1024 + vl;
    k.MR[g] = *(lds_u32x4)p; k.MI[g] = *(lds_u32x4)(p + 4096); k.T[g] = *(lds_u32x4)(p + 8192);
  }
  __builtin_amdgcn_s_sleep(24);
  { const int uu = 2 * w + h; wait_flags<12>(c, OFF_FA + ((j >> 3) * 256 + 128 * (uu & 1) + 32 * (uu >> 1) + r) * 8, true, 2); }
  const int so0 = 4 * j * 32768 + w * 8192;
  static_for<16>([&](auto N_) {
    constexpr int n = decltype(N_)::value;
    if constexpr ((n & 3) == 0) m0n<(n >> 3) * 32768 + ((n >> 2) & 1) * 4096>(c.wb8);
    dma_s<(n & 3) * 1024>(vl, c.rws, so0 + ((n >> 3) ? OFF_WT : OFF_X1) + ((n >> 2) & 1) * 4096);
  });
  if (j == 0) {
#pragma unroll
    for (int q = 0; q < 4; ++q) { setm0(c.Lb + C_X5 + q * 4096 + w * 1024); dma_s<0>(vl, c.rws, OFF_X5 + q * 4096 + w * 1024); }
  }
  const int sw = (r >> 1) & 7, nrd = (j == 0 && w == 0) ? 5 : 4;
  lds_char po = c.L + vl; int oso = 0, vst = OOB;
#pragma unroll 1
  for (int rd = 0; rd < nrd; ++rd) {
    const bool extra = rd == 4;
    HSTAMP(c, 32 + 4 * (rd & 3));
    wait_vm<0>();
    if (!extra) bar();
    HSTAMP(c, 4 * (rd & 3));
    if (rd == 0) STAMP(1);
    const lds_char B = c.L + (rd & 1) * 65536;
    const lds_char pa = extra ? c.L + C_X5 + vl : B + lane * 128 + (((2 * w) ^ sw) << 4);
    const lds_char pb = extra ? c.L + C_X5 + 4096 + vl : B + lane * 128 + (((2 * w + 1) ^ sw) << 4);
    const int vld = rd < 3 ? vl : OOB, sn = so0 + (rd + 1) * 32768;
    const unsigned mb = rd < 3 ? c.wb8 + ((rd + 1) & 1) * 65536 : 0x30000u;
    u32x4 t[2];
    col_pair(k, pa, pb, extra ? 1024 : 8192, extra ? 8192 : 32768, [&](auto N_) {
      constexpr int n = decltype(N_)::value;
      if constexpr (n == 16 || n == 26 || n == 44) { HSTAMP(c, 4 * (rd & 3) + (n == 16 ? 1 : n == 26 ? 2 : 3)); HSTAMP(c, 32 + 4 * (rd & 3) + (n == 16 ? 1 : n == 26 ? 2 : 3)); }
      if constexpr (n >= 8 && n < 24 && (n & 1) == 0) t[(n >> 1) & 1] = *(lds_u32x4)(po + ((n - 8) >> 1) * 1024);
      if constexpr (n >= 11 && n < 27 && (n & 1)) st16(t[((n - 3) >> 1) & 1], c.rws, vst, oso + ((n - 11) >> 1) * 1024);
      if constexpr (n < 16 || (n >= 26 && n < 42)) {
        constexpr int e = n < 16 ? n : n - 26, d = (e >> 1) + (n < 16 ? 8 : 0);
        if constexpr ((e & 7) == 0) m0q<(d >> 3) * 32768 + ((d >> 2) & 1) * 4096>(mb);
        if constexpr (e & 1) dma_s<(d & 3) * 1024>(vld, c.rws, sn + ((d >> 3) ? OFF_WT : OFF_X1) + ((d >> 2) & 1) * 4096);
      }
    });
    po = (extra ? c.L + C_X5 : B + w * 8192) + vl; oso = extra ? OFF_Y5 : OFF_YB + so0 + rd * 32768; vst = vl;
    if (rd == 3) bar();
  }
#pragma unroll
  for (int m = 0; m < 8; ++m) st16(*(lds_u32x4)(po + m * 1024), c.rws, vl, oso + m * 1024);
  if (nrd == 5) st8(c, c.tlo, c.thi + 0x9e3779b1u, OFF_EP);
  wait_vm<0>();
  bar();
  publish(c, OFF_FY + (w * 32 + j) * 8, 1);
}

constexpr int PD = 4, NVF = 40, NMIG = 40;
constexpr int nh(int HS) { return HS < 16 ? 32 : 28; }
constexpr int bu(int HS) { return nh(HS) - PD - 1; }
constexpr int mish_t0(int HS, int v) { return 4 + (v * (HS < 16 ? 98 : 82)) / 15; }
constexpr int stream_piece(int HS, int U) {
  if (HS + 2 > 31) return -1;
  for (int d = 0; d < nh(HS + 2) / 4; ++d) if (U == (HS < 16 ? (7 * d) / 3 : HS == 19 ? (3 * d) / 2 : 3 * d)) return d;
  return -1;
}
constexpr bool ev_stream(int HS, int U) { return stream_piece(HS, U) >= 0; }
constexpr int w2_piece(int HS, int U) { return HS < 2 || HS > 15 ? -1 : U == 19 ? 0 : U == 22 ? 1 : U == 24 ? 2 : U == 27 ? 3 : -1; }
constexpr bool ev_w2(int HS, int U) { return w2_piece(HS, U) >= 0; }
constexpr bool ev_spec(int HS, int U) { return HS <= 1 && U >= 18 && (U & 1) == 0 && U <= (HS == 0 ? 26 : 24); }
constexpr bool ev_hst(int HS, int U) {
  if (HS < 2 || HS > 17) return false;
  const int cu = (HS & 1) * nh(HS) + U;
  return cu == (mish_t0(HS, 7) + 14) / 2 || cu == (mish_t0(HS, 15) + 14) / 2;
}
constexpr bool ev_pst(int HS, int U) { return HS >= 18 && HS <= 28 && (HS & 1) == 0 && (U == 7 || U == 13); }
constexpr bool ev_poll(int HS, int U) { return U == 21 && (HS == 12 || HS == 17 || HS == 18 || HS == 24 || HS == 28 || HS == 30); }
constexpr int ev_irx(int HS, int U) { return HS == 20 && U >= 2 && U <= 23 && U % 3 == 2; }
constexpr int ev_irld(int HS, int U) { return (HS == 19 && U >= 12 && U <= 22 && (U & 1) == 0) + ev_irx(HS, U); }
constexpr bool ev_yxst(int HS, int U) { return HS == 26 && U >= 15 && U <= 21 && (U & 1); }
constexpr bool ev_fin(int HS, int U) { return HS == 30 && U < 20 && (U & 1) == 0; }
constexpr bool pub_h(int HS) { return HS >= 4 && HS <= 18 && (HS & 1) == 0; }
constexpr bool pub_p(int HS) { return HS >= 19 && HS <= 29 && (HS & 1); }
constexpr bool pub_a(int HS) { return HS == 2 || HS == 27; }
constexpr int nvm(bool CV, int HS, int U) {
  return ev_stream(HS, U) + ev_w2(HS, U) + ev_spec(HS, U) + (!CV && ev_hst(HS, U)) + ev_pst(HS, U) + ev_poll(HS, U) + ev_irld(HS, U) + ev_yxst(HS, U) + ev_fin(HS, U);
}
constexpr int since_barrier(bool CV, int HS) {
  int n = 0;
  if (HS >= (CV ? 17 : 1)) { n += (!CV && pub_h(HS - 1)) + pub_p(HS - 1) + pub_a(HS - 1); for (int U = bu(HS - 1) + 1; U < nh(HS - 1); ++U) n += nvm(CV, HS - 1, U); }
  for (int U = 0; U <= bu(HS); ++U) n += nvm(CV, HS, U);
  return n;
}
struct Irow {
  unsigned ya[8], yb[8], y5[2], z[4], k0;
  u32x2 xr[2][4];
  u32x4 D[2][2], M[2], T, a;
  f32x16 Vr, Vi, y;
  float nyq[2];
  u32x4 o[4];
  lds_char py, po, pt, pc;
  rsrc_t rx; int vy, vx, vc;
};
struct Gm {
  f32x4 acc[4], pend[4], bias[2];
  float e[4];
  unsigned pk[4], mig[4];
  u32x4 b[PD];
  s16x4 wl;
  u32x2 fv;
  u32x4 fin[16], sel[2];
  lds_char p0, p2;
  rsrc_t rout;
  int vl, vh, vout, src1[2], src2[2], hdst, pdst, psrc, ysrc, osrc, w2row;
  unsigned fh, fp, fa, pollh, polly, pollf, needf; bool acth, actf;
};
DI int gm_cc(const Ctx& c, int t) { return (2 * c.kq + 2 + t) & 7; }
template <int HS, int D> DI void gm_m0(const Ctx& c) { m0q<(HS % 3) * 32768 + D * 1024>(HS < 16 ? c.wb8 : c.wb7); }
template <int HS, int D> DI void gm_dma(const Ctx& c, const Gm& m) {
  dma_s<(D & 3) * 1024>(m.vl, c.rws, (HS < 16 ? m.src1[HS & 1] + gm_cc(c, HS >> 1) * 65536 : m.src2[HS & 1] + gm_cc(c, (HS - 16) >> 1) * 229376) + (D & ~3) * 1024);
}
template <int HS> DI void gm_fetch(const Ctx& c, const Gm& m) {
  static_for<nh(HS) / 4>([&](auto D_) { constexpr int D = decltype(D_)::value; if constexpr ((D & 3) == 0) { gm_m0<HS, D>(c); asm volatile("s_nop 0"); } gm_dma<HS, D>(c, m); });
}
template <int HS, int U> DI u32x4 gm_frag(const Gm& m) { return *(lds_u32x4)((HS % 3 == 2 ? m.p2 : m.p0) + (HS % 3 == 1 ? 32768 : 0) + U * 1024); }

template <int K> DI void irow_step(const Ctx& c, Irow& q, unsigned f) {
  constexpr int K_RD = 0, K_BF = 18, K_ROW = 54, ROWLEN = 104;
  if constexpr (K == 0) { PIN(q.y5[0]); PIN(q.y5[1]); }
  if constexpr (K == 56) {
#pragma unroll
    for (int s = 0; s < 2; ++s) { for (int g = 0; g < 4; ++g) PIN(q.xr[s][g]); }
  }
  if constexpr (K >= K_RD && K < K_RD + 16) {
    constexpr int rho = (K - K_RD) >> 1, off = 512 * ((rho & 3) + 8 * (rho >> 2));
    if constexpr (K & 1) q.ya[rho] = *(lds_u32)(q.py + off); else q.yb[rho] = *(lds_u32)(q.py + 8192 + off);
  }
  if constexpr (K == 16) { q.y5[1] = cmulc(q.y5[1], f); PIN(q.y5[1]); }
  if constexpr (K == 17) { const float a = h_hi(q.y5[0]) * (1.f / 64), b = h_hi(q.y5[1]) * (1.f / 64); q.nyq[0] = a + b; q.nyq[1] = a - b; PIN(q.nyq[0]); PIN(q.nyq[1]); }
  if constexpr (K >= K_BF && K < K_BF + 32) {
    constexpr int rho = (K - K_BF) >> 2, ph = (K - K_BF) & 3;
    if constexpr (ph == 0) q.z[rho & 1] = i_cma(q.yb[rho], f);
    if constexpr (ph == 1 && rho > 0) q.D[0][(rho - 1) >> 2][(rho - 1) & 3] = i_add(q.ya[rho - 1], q.yb[rho - 1]);
    if constexpr (ph == 2) q.yb[rho] = i_cmb(q.yb[rho], f, q.z[rho & 1]);
    if constexpr (ph == 3 && rho > 0) q.D[1][(rho - 1) >> 2][(rho - 1) & 3] = i_sub(q.ya[rho - 1], q.yb[rho - 1]);
  }
  if constexpr (K == K_BF + 32) q.D[0][1][3] = i_add(q.ya[7], q.yb[7]);
  if constexpr (K == K_BF + 33) q.D[1][1][3] = i_sub(q.ya[7], q.yb[7]);
  if constexpr (K == K_BF + 34) q.D[0][0][0] = i_mul(q.D[0][0][0], q.k0);
  if constexpr (K == K_BF + 35) q.D[1][0][0] = i_mul(q.D[1][0][0], q.k0);
  if constexpr (K >= K_ROW - 4 && K < K_ROW + 2 * ROWLEN) {
    constexpr int s = (K - K_ROW + 4) / ROWLEN > 1 ? 1 : (K - K_ROW + 4) / ROWLEN, t = K - K_ROW - s * ROWLEN;
    if constexpr (t == -4) q.M[0] = *(lds_u32x4)(q.pt);
    if constexpr (t == -3) q.M[1] = *(lds_u32x4)(q.pt + 4096);
    if constexpr (t == -2) q.T = *(lds_u32x4)(q.pt + 1024);
    if constexpr (t == -1) q.a = *(lds_u32x4)(q.pt + 4096 + 1024);
    if constexpr (t == 2) mhz(q.Vr, q.D[s][0], q.M[0]);
    if constexpr (t == 3) mhz(q.Vi, q.D[s][0], q.M[1]);
    if constexpr (t == 4) mh(q.Vr, q.D[s][1], q.T);
    if constexpr (t == 5) mh(q.Vi, q.D[s][1], q.a);
    if constexpr (t >= 6 && t < 22) {
      constexpr int v = t - 6;
      const unsigned xw = q.xr[s][v >> 2][(v >> 1) & 1];
      float y0 = (v & 1) ? bf_hi(xw) - q.nyq[s] : bf_lo(xw) + q.nyq[s];
      PIN(y0); q.y[v] = y0;
    }
    if constexpr (t >= 18 && t < 18 + 60) {
      constexpr int g = (t - 18) / 15, j = (t - 18) % 15;
      if constexpr (j == 0) q.T = *(lds_u32x4)(q.pt + 8192 + g * 1024);
      if constexpr (j == 1) q.M[g & 1] = *(lds_u32x4)(q.pt + 4096 + g * 1024);
    }
    if constexpr (t >= 22 && t < 22 + 60) {
      constexpr int g = (t - 22) / 15, j = (t - 22) % 15;
      if constexpr (j < 4) q.z[j] = i_cvt(q.Vr[4 * g + j], q.Vi[4 * g + j]);
      if constexpr (j >= 3 && j < 7) q.a[j - 3] = i_cma(q.z[j - 3], q.T[j - 3]);
      if constexpr (j >= 7 && j < 11) q.a[j - 7] = i_cmb(q.z[j - 7], q.T[j - 7], q.a[j - 7]);
      if constexpr (j == 13) mh(q.y, q.a, q.M[g & 1]);
    }
    if constexpr (t >= 86 && t < 98) {
      constexpr int g = (t - 86) / 3, j = (t - 86) % 3;
      if constexpr (j < 2) { q.z[j] = pk_bf16(q.y[4 * g + 2 * j], q.y[4 * g + 2 * j + 1]); PIN(q.z[j]); }
      else *(lds_u32x2)(lxor(q.po, s << 4) + g * 128) = u32x2{q.z[0], q.z[1]};
    }
  }
}
constexpr int irow_k(int HS, int U, int ph) {
  return HS < 20 ? -1 : 2 * ((HS - 20) * 28 + U - (bu(20) + 1)) + ph;
}
static_assert(irow_k(25, bu(25) - 6, 0) >= 54 + 104 + 98, "the output image must be complete before barrier 25");

template <bool CV, int HS, int U> DI void gm_unit(const Ctx& c, Gm& m, u32x4 (&A)[64], u32x4 (&W)[64], Stat& s2, const Spec& sp, Irow& q) {
  constexpr int NH = nh(HS), CU = (HS & 1) * NH + U, ks = CU >> 1, rt = U & 1, GU = U % PD;
  static_assert(NH % PD == 0, "");
  if constexpr (CU == 0 && HS >= (CV ? 18 : 2)) { for (int j = 0; j < 4; ++j) m.pend[j] = m.acc[j]; }
  const u32x4 b = m.b[GU];
  auto mma = [&](auto CT_) {
    constexpr int ct = decltype(CT_)::value, f = 2 * ks + ct;
    f32x4& acc = m.acc[2 * rt + ct];
    if constexpr (HS < 16) {
      if constexpr (CU < 2) mf16c(acc, A[f], b, m.bias[ct]); else if constexpr (rt == 0 && ct == 0) mf16w(acc, A[f], b, m.b[(U + 1) % PD]); else mf16(acc, A[f], b);
    } else if constexpr (CV || f >= NVF || (f < NMIG && HS >= 20)) {
      if constexpr (CU < 2) mf16z(acc, W[f], b); else if constexpr (rt == 0 && ct == 0) mf16w(acc, W[f], b, m.b[(U + 1) % PD]); else mf16(acc, W[f], b);
    } else {
      if constexpr (CU < 2) mf16vz(acc, W[f], b); else if constexpr (rt == 0 && ct == 0) mf16vw(acc, W[f], b, m.b[(U + 1) % PD]); else mf16v(acc, W[f], b);
    }
    GAP();
  };
  auto side = [&](auto PH_) {
    constexpr int ph = decltype(PH_)::value;
    if constexpr (!CV && HS >= 2 && HS <= 17) {
      static_for<16>([&](auto V_) {
        constexpr int v = decltype(V_)::value, t = 2 * CU + ph - mish_t0(HS, v);
        if constexpr (t >= 0 && (t & 1) == 0) {
          constexpr int k = t / 2;
          float& x = ((float*)&m.pend[v >> 2])[v & 3];
          if constexpr (k == 0) { m.e[v & 3] = __builtin_amdgcn_exp2f(x); PIN(m.e[v & 3]); }
          if constexpr (k == 1) { m.e[v & 3] += 1.f; PIN(m.e[v & 3]); }
          if constexpr (k == 2) { m.e[v & 3] = m.e[v & 3] * m.e[v & 3] + 1.f; PIN(m.e[v & 3]); }
          if constexpr (k == 3) { m.e[v & 3] = __builtin_amdgcn_rcpf(m.e[v & 3]); PIN(m.e[v & 3]); }
          if constexpr (k == 4) { m.e[v & 3] = m.e[v & 3] * -1.38629436f + 0.69314718f; PIN(m.e[v & 3]); }
          if constexpr (k == 5) { x *= m.e[v & 3]; PIN(x); }
          if constexpr (k == 6 && (v & 1)) { m.pk[(v >> 1) & 3] = pk_bf16(m.pend[v >> 2][(v & 3) - 1], x); PIN(m.pk[(v >> 1) & 3]); }
          if constexpr (k == 7 && (v & 7) == 7) {
            static_assert(ev_hst(HS, U), "");
            st16(u32x4{m.pk[0], m.pk[1], m.pk[2], m.pk[3]}, c.rws, m.vl, m.hdst + gm_cc(c, (HS >> 1) - 1) * 229376 + (v >> 3) * 1024);
          }
        }
      });
    }
    if constexpr (!CV && HS >= 4 && HS <= 17 && ph == 1 && U >= 5 && U < 21 && (U & 1)) {
      constexpr int st = HS - 4, j = (U - 5) / 2, fr = j >> 1, off = (st % 3) * 16384 + (fr >> 1) * 8192 + (fr & 1) * 8;
      if constexpr ((j & 1) == 0) m.wl = trr(s2.p + off); else W[4 * st + fr] = cat(m.wl, trr(s2.p + off + 1024));
    }
    if constexpr (HS >= 18 && HS <= 28 && (HS & 1) == 0 && ph == 1 && U >= 3 && U < 14 && U != 8) {
      constexpr int k = U < 8 ? U - 3 : U - 4, hf = k / 5, j = k % 5;
      if constexpr (j < 4) { m.pk[j] = pk_bf16(m.pend[2 * hf + (j >> 1)][2 * (j & 1)], m.pend[2 * hf + (j >> 1)][2 * (j & 1) + 1]); PIN(m.pk[j]); }
      else { static_assert(ev_pst(HS, U), ""); st16(u32x4{m.pk[0], m.pk[1], m.pk[2], m.pk[3]}, c.rws, m.vl, m.pdst + gm_cc(c, ((HS - 16) >> 1) - 1) * 32768 + hf * 1024); }
    }
    if constexpr (!CV && HS >= 18 && (HS - 18) * 28 + U >= 4 && (HS - 18) * 28 + U < 4 + 2 * NMIG) {
      constexpr int k = 2 * ((HS - 18) * 28 + U - 4) + ph;
      m.mig[k & 3] = v2a(W[k >> 2][k & 3]);
      if constexpr ((k & 3) == 3) { W[k >> 2] = u32x4{m.mig[0], m.mig[1], m.mig[2], m.mig[3]}; asm volatile("" : "+a"(W[k >> 2])); }
    }
    if constexpr (irow_k(HS, U, ph) >= 0) irow_step<irow_k(HS, U, ph)>(c, q, sp.f);
    if constexpr (HS == 26 && ph == 0 && U >= 11 && U <= 17 && (U & 1)) {
      constexpr int j = (U - 11) / 2; q.o[j] = *(lds_u32x4)(lxor(q.pc, j << 5) + j * 1024);
    }
    if constexpr (HS == 31 && U >= 6 && U < 16) {
      constexpr int j = U - 6;
      if constexpr (j < 8) mf16v(m.pend[2 * (j & 1) + ph], m.sel[ph], m.fin[j]); else mf16v(m.acc[2 * ((j & 1) ^ 1) + ph], m.sel[ph], m.fin[8 + ((j & 1) ^ 1)]);
    }
    if constexpr (HS == 31 && U >= 17 && U < 27 && ph == 1) {
      constexpr int k = U - 17, hf = k / 5, j = k % 5;
      if constexpr (j < 4) { m.pk[j] = pk_bf16(m.pend[2 * hf + (j >> 1)][2 * (j & 1)], m.pend[2 * hf + (j >> 1)][2 * (j & 1) + 1]); PIN(m.pk[j]); }
      else __builtin_amdgcn_raw_buffer_store_b128(u32x4{m.pk[0], m.pk[1], m.pk[2], m.pk[3]}, m.rout, m.vout, m.osrc + hf * 4096, 0);
    }
  };
  mma(std::integral_constant<int, 0>{});
  u32x4 nb = b;
  if constexpr (U + PD < NH) nb = gm_frag<HS, U + PD>(m); else if constexpr (HS < 31) nb = gm_frag<HS + 1, U + PD - NH>(m);
  if constexpr (ev_stream(HS, U) && (stream_piece(HS, U) & 3) == 0) gm_m0<HS + 2, stream_piece(HS, U)>(c);
  if constexpr (w2_piece(HS, U) == 0) m0q<L_ST + ((HS - 2) % 3) * 16384>(c.wb4);
  if constexpr (HS == 19 && ev_irld(HS, U) && U < 20) m0q<L_Y + (U - 12) * 512>(c.wb4);
  side(std::integral_constant<int, 0>{});
  GAP();
  mma(std::integral_constant<int, 1>{});
  m.b[GU] = nb;
  if constexpr (ev_stream(HS, U)) gm_dma<HS + 2, stream_piece(HS, U)>(c, m);
  if constexpr (ev_w2(HS, U)) dma_w<w2_piece(HS, U) * 1024>(s2.vo[w2_piece(HS, U)], s2.rw, s2.so);
  if constexpr (HS >= 2 && HS <= 15 && U == 29) { s2.so += s2.step; s2.so -= s2.so >= s2.wrap ? s2.wrap : 0; asm volatile("" : "+s"(s2.so)); }
  if constexpr (ev_spec(HS, U)) spec_store(c, sp, 5 * HS + (U - 18) / 2);
  if constexpr (ev_poll(HS, U)) ldf(m.fv, HS == 17 ? m.polly : HS >= 28 ? m.pollf : m.pollh, c.rws);
  if constexpr (HS == 19 && ev_irld(HS, U)) {
    constexpr int j = (U - 12) / 2;
    if constexpr (j < 4) dma_s<0>(q.vy, c.rws, OFF_YB + (32 * c.w + 8 * j) * 32768 + (c.u * 32 + c.lo) * 128);
    else lds1(q.y5[j - 4], 0, c.rws, OFF_Y5 + (j - 4) * 4096 + (c.u * 32 + c.lo) * 16 + c.w * 4);
  }
  if constexpr (ev_irx(HS, U)) { constexpr int j = U / 3; ldx2<16 * (j & 3)>(q.xr[j >> 2][j & 3], q.vx, q.rx, (j >> 2) * 1024 * 2048); }
  if constexpr (ev_yxst(HS, U))
    st16(q.o[(U - 15) / 2], c.rws, q.vc, OFF_YX + (c.blkp >> 2) * 65536 + ((c.blkp >> 1) & 1) * 1024 + (c.blkp & 1) * 128 + c.w * 16384 + ((U - 15) / 2) * 4096);
  if constexpr (ev_fin(HS, U)) {
    constexpr int j = U / 2;
    if constexpr (j < 6) lds4(m.fin[j], m.vl, c.rws, m.psrc + ((c.kq + 1 + (j >> 1)) & 3) * 8192 + (j & 1) * 1024);
    else lds4(m.fin[j], m.vl, c.rws, m.ysrc + ((j - 6) >> 1) * 65536 + (j & 1) * 1024);
  }
  if constexpr (HS == 31 && U >= 5 && U < 11) lds4(m.fin[10 + U - 5], m.vl, c.rws, m.psrc + 32768 + ((c.kq + 1 + ((U - 5) >> 1)) & 3) * 8192 + ((U - 5) & 1) * 1024);
  side(std::integral_constant<int, 1>{});
  if constexpr (U == bu(HS) && HS < 31) {
    GAP();
    if constexpr (!CV && HS >= 4 && HS <= 17) {
      constexpr int st = HS - 4;
      if constexpr (4 * st < NVF) asm volatile("" :: "v"(W[4 * st]), "v"(W[4 * st + 1]), "v"(W[4 * st + 2]), "v"(W[4 * st + 3]));
      else asm volatile("" : "+a"(W[4 * st]), "+a"(W[4 * st + 1]), "+a"(W[4 * st + 2]), "+a"(W[4 * st + 3]));
    }
    HSTAMP(c, 32 + HS);
    wait_vm<since_barrier(CV, HS)>();
    bar_nowait();
    HSTAMP(c, HS);
    if constexpr (!CV && pub_h(HS)) publish(c, m.fh, HS / 2 - 1);
    if constexpr (pub_p(HS)) publish(c, m.fp, (HS - 17) / 2);
    if constexpr (pub_a(HS)) publish(c, m.fa, HS == 2 ? 2 : 3);
    if constexpr (HS == 13 || HS == 19 || HS == 25) {
      constexpr unsigned need = HS == 13 ? 3 : HS == 19 ? 6 : 8;
      PIN(m.fv); if (!flag_ok(c, m.fv, m.acth, need)) wait_flags(c, m.pollh, m.acth, need);
    }
    if constexpr (HS == 29) { PIN(m.fv); if (!flag_ok(c, m.fv, m.actf, m.needf)) wait_flags(c, m.pollf, m.actf, m.needf); }
  }
  if constexpr (HS == 18 && U == 20) {
    GAP(); wait_vm<9>(); PIN(m.fv); if (!flag_ok(c, m.fv, true, 1)) wait_flags(c, m.polly, true, 1);
  }
  if constexpr (HS == 31 && U == 4) {
    GAP(); wait_vm<0>();
#pragma unroll
    for (int j = 0; j < 10; ++j) PIN(m.fin[j]);
    PIN(m.fv); if (!flag_ok(c, m.fv, m.actf && c.lane < 4, m.needf + 1)) wait_flags(c, m.pollf, m.actf && c.lane < 4, m.needf + 1);
  }
  GAP();
}
template <bool CV, int H0, int H1> DI void gm_run(const Ctx& c, Gm& m, u32x4 (&A)[64], u32x4 (&W)[64], Stat& s2, const Spec& sp, Irow& q) {
  static_for<H1 - H0>([&](auto H_) { constexpr int HS = H0 + decltype(H_)::value; static_for<nh(HS)>([&](auto U_) { gm_unit<CV, HS, decltype(U_)::value>(c, m, A, W, s2, sp, q); }); });
}
template <bool CV> DI void gemm2(const Ctx& c, Gm& m, u32x4 (&A)[64], u32x4 (&W)[64], Stat& s2, const Spec& sp, const char* x, char* out STAMP_ARG) {
  const int n2 = c.lo >> 2, w = c.w;
  Irow q;
  m.fp = OFF_FP + (((c.mt * 8 + n2) * 4 + w) * 4 + c.kq) * 8;
  m.pdst = OFF_PT + (c.mt * 8 + n2) * 262144 + c.kq * 8192 + w * 2048;
  { int ln = c.lane; PIN(ln); m.polly = OFF_FY + ((c.u >> 1) * 32 + (ln & 31)) * 8; }
  STAMP(13);
  gm_run<CV, 16, 19>(c, m, A, W, s2, sp, q);
  {
    int ln = c.lane; PIN(ln);
    const int r = ln & 31, h = ln >> 5;
    q.py = c.L + L_Y + (r + 128 * h) * 16 + w * 4;
    q.po = c.L + L_O + r * 512 + (((2 * w) ^ (r & 7)) << 4) + 8 * h;
    q.pt = c.L + L_TB + ln * 16;
    q.vy = (ln >> 3) * 32768 + (((ln & 7) ^ ((c.lo >> 1) & 7)) << 4);
    q.vx = r * 64 + h * 8;
    q.rx = mk_rsrc(x + (size_t)(32 * (4 * c.u + w) + c.lo) * 2048, 1025 * 2048);
    q.k0 = ln == 0 ? 0x38003800u : 0x3c003c00u;
  }
  gm_run<CV, 19, 26>(c, m, A, W, s2, sp, q);
  {
    int ln = c.lane; PIN(ln);
    q.pc = c.L + L_O + w * 4096 + (ln >> 3) * 128 + (((ln & 7) ^ (ln >> 5)) << 4);
    q.vc = (ln >> 5) * 2048 + ((ln >> 3) & 3) * 256 + (ln & 7) * 16;
  }
  gm_run<CV, 26, 28>(c, m, A, W, s2, sp, q);
  {
    int ln = c.lane; PIN(ln);
    unsigned fa = OFF_FP + ((c.mt * 8 + n2) * 4 + w) * 32, fb = OFF_FA + ((n2 >> 1) * 256 + c.mt * 32 + c.kq * 8) * 8;
    asm volatile("" : "+s"(fa), "+s"(fb));
    m.pollf = (ln < 4 ? fa : fb) + (ln & 7) * 8;
    m.actf = ln < 4 ? ln != c.kq : ln < 16;
    m.needf = ln < 4 ? 2 * ((c.kq - ln - 1) & 3) + 1 : 3;
    m.psrc = OFF_PT + (c.mt * 8 + n2) * 262144 + 2 * c.kq * 32768 + w * 2048;
    m.ysrc = OFF_YX + (c.mt * 8 + 2 * c.kq) * 65536 + (n2 * 4 + w) * 2048;
  }
  gm_run<CV, 28, 29>(c, m, A, W, s2, sp, q);
  {
    int ln = c.lane; PIN(ln);
    const int mm = ln & 15, g = ln >> 4;
    m.rout = mk_rsrc(out, 4u << 20);
    m.vout = (32 * ((mm >> 1) & 3) + (mm >> 3) + 1024 * (mm & 1)) * 2048 + g * 16;
    m.osrc = (128 * (2 * (c.mt & 3) + (c.mt >> 2)) + 8 * c.kq) * 2048 + n2 * 256 + w * 64;
    const unsigned one = g == (mm >> 2) ? ((mm & 1) ? 0x3f800000u : 0x3f80u) : 0u, lo2 = (mm & 2) ? 0u : one, hi2 = (mm & 2) ? one : 0u;
    m.sel[0] = u32x4{lo2, hi2, 0u, 0u}; m.sel[1] = u32x4{0u, 0u, lo2, hi2};
    PIN(m.sel[0]); PIN(m.sel[1]); PIN(m.vout);
  }
  gm_run<CV, 29, 32>(c, m, A, W, s2, sp, q);
  STAMP(14);
  wait_vm<2>();
#pragma unroll
  for (int j = 10; j < 16; ++j) { PIN(m.fin[j]); }
#pragma unroll
  for (int j = 0; j < 6; ++j) { mf16v(m.acc[2 * (j & 1)], m.sel[0], m.fin[10 + j]); mf16v(m.acc[2 * (j & 1) + 1], m.sel[1], m.fin[10 + j]); }
  asm volatile("s_nop 7" : "+v"(m.acc[0]), "+v"(m.acc[1]), "+v"(m.acc[2]), "+v"(m.acc[3]));
#pragma unroll
  for (int rt = 0; rt < 2; ++rt)
    __builtin_amdgcn_raw_buffer_store_b128(u32x4{pk_bf16(m.acc[2 * rt][0], m.acc[2 * rt][1]), pk_bf16(m.acc[2 * rt][2], m.acc[2 * rt][3]),
                                                 pk_bf16(m.acc[2 * rt + 1][0], m.acc[2 * rt + 1][1]), pk_bf16(m.acc[2 * rt + 1][2], m.acc[2 * rt + 1][3])},
                                           m.rout, m.vout, m.osrc + 8192 + rt * 4096, 0);
  STAMP(15); STAMP_FLUSH(c.ws, 0); HSTAMP_FLUSH(c);
}

__global__ __launch_bounds__(256, 1) void k_fused(const char* __restrict__ x, const char* __restrict__ wr, const char* __restrict__ wi, const char* __restrict__ w1,
                                                  const char* __restrict__ b1, const char* __restrict__ w2, char* __restrict__ out, char* __restrict__ ws) {
  extern __shared__ __attribute__((aligned(16))) char smem[];
  STAMP_DECL; STAMP(0);
  Ctx c;
  c.ws = ws; c.rws = mk_rsrc(ws, WS_USED); c.L = (lds_char)smem; c.Lb = (unsigned)(size_t)c.L;
  c.lane = threadIdx.x & 63; c.w = rfl(threadIdx.x >> 6);
  c.u = blockIdx.x & 7; c.lo = blockIdx.x >> 3; c.blkp = 128 * (c.u & 1) + 32 * (c.u >> 1) + c.lo;
  c.kq = c.u >> 1; c.mt = (c.u & 1) * 4 + (c.lo & 3); c.rot = ROT ? c.lo & 3 : 0;
  c.v0 = c.lane == 0 ? 0 : OOB;
  c.wb4 = c.Lb + c.w * 4096; c.wb7 = c.Lb + c.w * 7168; c.wb8 = c.Lb + c.w * 8192;
  const int lane = c.lane, w = c.w, n2 = c.lo >> 2;
  FrontIn in;
  front_loads(c, in, x);
  { unsigned long long pc; unsigned len;
    asm volatile("s_getpc_b64 %0\nfused_begin:\n\ts_mov_b32 %1, fused_end-fused_begin" : "=s"(pc), "=s"(len));
    const rsrc_t rcode = mk_rsrc((const void*)(pc & ~127ull), len);
    setm0(c.Lb + R_PF + w * 256);
    asm volatile("buffer_load_dword %0, %1, %2 offen lds" :: "v"(lane < 8 ? lane * 16384 : OOB), "s"(rcode), "s"((c.lo * 4 + w) * 128) : "memory"); }
  u32x4 A[64], W[64];
  Spec sp; Gm m;
  const int n1 = c.kq * 7 + n2;
  const char* w2t = w2 + (size_t)c.kq * 896 * 2048 + n2 * 256;
  Stat st = stat_make(c, n2 < 7 ? w1 + n1 * 256 : w2t, n2 < 7 ? 7168 : 2048, n2 < 7 ? 1024 : 896, R_ST);
  phase_front(c, st, A, sp, in, wr, wi STAMP_PASS);
  m.vl = lane * 16; m.p0 = c.L + lane * 16; m.p2 = c.L + 65536 + lane * 16;
#pragma unroll
  for (int hh = 0; hh < 2; ++hh) {
    const int e8 = (4 * hh + w + 2 * c.rot) & 7;
    m.src1[hh] = OFF_XN + c.mt * 524288 + e8 * 8192;
    m.src2[hh] = OFF_H + c.mt * 8 * 229376 + c.kq * 57344 + e8 * 7168;
  }
  m.fa = OFF_FA + (w * 256 + c.blkp) * 8;
  m.fh = OFF_FH + ((c.mt * 4 + c.kq) * 32 + n2 * 4 + w) * 8;
  m.pollh = OFF_FH + ((c.mt * 4 + c.kq) * 32 + (lane & 31)) * 8; m.acth = (lane & 31) < 28;
  m.hdst = OFF_H + c.mt * 8 * 229376 + (n1 * 4 + w) * 2048;
  const unsigned fxn = OFF_FA + ((((w >> 1) + c.rot + 2 * (lane >> 5)) & 3) * 256 + c.mt * 32 + (lane & 31)) * 8;
  u32x4 bv;
  ldf(m.fv, fxn, c.rws);
  { const rsrc_t rb = mk_rsrc(b1, 7168); asm volatile("s_nop 4" ::: "memory"); ldx4<0>(bv, (n1 * 128 + 32 * w + 8 * (lane >> 4)) * 2, rb); }
  fbar<5, 10>(A);
  STAMP(9);
  front_tail(c, st, A, sp);
  fbar<6, 8>(A);
  STAMP(10);
  PIN(m.fv); PIN(bv);
  fstream<6>(c, st, A);
  if (n2 < 7) {
    if (!flag_ok(c, m.fv, true, 1)) wait_flags(c, fxn, true, 1);
    gm_fetch<0>(c, m);
  }
  table_build(c);
  m.bias[0] = f32x4{bf_lo(bv[0]), bf_hi(bv[0]), bf_lo(bv[1]), bf_hi(bv[1])} * 1.44269504f;
  m.bias[1] = f32x4{bf_lo(bv[2]), bf_hi(bv[2]), bf_lo(bv[3]), bf_hi(bv[3])} * 1.44269504f;
  PIN(m.bias[0]); PIN(m.bias[1]);
  if (n2 == 7) wait_vm<0>();
  fbar<7, 8>(A);
  fstream<7>(c, st, A);
  if (n2 < 7) gm_fetch<1>(c, m);
  stat_pin(A, 14, 16);
  wait_vm<8>();
  bar();
  STAMP(11);
  Stat s2 = stat_make(c, w2t, 2048, 896, L_ST);
  if (n2 == 7) {
#pragma unroll
    for (int j = 0; j < 9; ++j) spec_store(c, sp, j);
    wait_vm<0>();
    publish(c, m.fa, 2);
    phase_cols(c, c.u * 4 + (c.lo & 3) STAMP_PASS);
    STAMP(12);
    wait_flags(c, m.pollh, m.acth, 3);
    gm_fetch<16>(c, m); gm_fetch<17>(c, m);
    wait_vm<7>(); bar();
#pragma unroll
    for (int j = 0; j < PD; ++j) m.b[j] = *(lds_u32x4)(m.p0 + 32768 + j * 1024);
    GAP();
    gemm2<true>(c, m, A, A, s2, sp, x, out STAMP_PASS);
    asm volatile("s_endpgm" ::: "memory");
#pragma unroll
    for (int j = 0; j < 9; ++j) asm volatile("" : "=v"(sp.v[j]));
    asm volatile("" : "=v"(sp.vo), "=v"(sp.vo5), "=v"(m.bias[0]), "=v"(m.bias[1]));
  }
#pragma unroll
  for (int j = 0; j < PD; ++j) m.b[j] = *(lds_u32x4)(m.p0 + j * 1024);
  GAP();
  { Irow q0; gm_run<false, 0, 16>(c, m, A, W, s2, sp, q0); }
  gemm2<false>(c, m, A, W, s2, sp, x, out STAMP_PASS);
  asm volatile("fused_end:");
}

extern "C" void kernel_launch(void* const* d_in, const int* in_sizes, int n_in, void* d_out, int out_size, void* d_ws, size_t ws_size, hipStream_t stream) {
  (void)in_sizes; (void)n_in; (void)out_size; (void)ws_size;
  (void)hipFuncSetAttribute((const void*)k_fused, hipFuncAttributeMaxDynamicSharedMemorySize, 163840);
  k_fused<<<256, 256, 163840, stream>>>((const char*)d_in[0], (const char*)d_in[1], (const char*)d_in[2], (const char*)d_in[4], (const char*)d_in[5], (const char*)d_in[6],
                                         (char*)d_out, (char*)d_ws);
}
```
